```python
import torch

def _softmax(x, axis=-1):
    return torch.softmax(x, dim=axis)
def _gelu(x):
    return torch.nn.functional.gelu(x, approximate="tanh")
def _silu(x):
    return torch.nn.functional.silu(x)
def _sigmoid(x):
    return torch.sigmoid(x)
def _layernorm(x, axis=-1, eps=1e-5):
    m = x.mean(dim=axis, keepdim=True)
    v = x.var(dim=axis, keepdim=True, unbiased=False)
    return (x - m) / torch.sqrt(v + eps)
def _scatter_add(tgt, idx, src, axis=0):
    return tgt.clone().index_add_(axis, idx, src)
def _segment_sum(src, seg, n):
    out = torch.zeros((n,) + src.shape[1:], dtype=src.dtype)
    return out.index_add_(0, seg, src)
def _pad(x, pw):
    flat = tuple(v for pair in reversed(pw) for v in pair)
    return torch.nn.functional.pad(x, flat)

kernel_name = 'parallel_sum__ssm_conv_s4__dense_activation_squared_relu__prompt'

def setup_inputs():

    _w = (lambda a: (lambda t: t.to(torch.bfloat16) if t.is_floating_point() else t)(torch.as_tensor(a)))
    def _A_setup():
        B, L, D, N = 1, 2048, 448, 256
        A_re = -(lambda _x: torch.exp(torch.as_tensor(_x)))((lambda *s: torch.randn(*s, dtype=torch.bfloat16) * 3.2)(D, N))
        A_im = torch.pi * (lambda *s: torch.randn(*s, dtype=torch.bfloat16) * 0.2)(D, N)
        B_re = (lambda *s: torch.randn(*s, dtype=torch.bfloat16) * 0.4)(D, N)
        B_im = (lambda *s: torch.randn(*s, dtype=torch.bfloat16) * 0.4)(D, N)
        C_re = (lambda *s: torch.randn(*s, dtype=torch.bfloat16) * 0.4)(D, N)
        C_im = (lambda *s: torch.randn(*s, dtype=torch.bfloat16) * 0.4)(D, N)
        log_dt = (lambda *s: torch.randn(*s, dtype=torch.bfloat16) * 0.2)(D) * 0.5 - 4.0
        Dskip = (lambda *s: torch.randn(*s, dtype=torch.bfloat16) * 0.4)(D)
        return {
            "u": _w((lambda *s: torch.randn(*s, dtype=torch.bfloat16) * 0.05)(B, L, D)),
            "A_re": _w(A_re),
            "A_im": _w(A_im),
            "B_re": _w(B_re),
            "B_im": _w(B_im),
            "C_re": _w(C_re),
            "C_im": _w(C_im),
            "log_dt": _w(log_dt),
            "Dskip": _w(Dskip),
        }
    _A_in = _A_setup()

    def _B_setup():
        B, S, D, F = 1, 2048, 448, 1792
        return {
            "x": _w((lambda *s: torch.randn(*s, dtype=torch.bfloat16) * 0.05)(B, S, D)),
            "w1": _w((lambda *s: torch.randn(*s, dtype=torch.bfloat16) * 0.05)(D, F)),
            "b1": _w((lambda *s: torch.randn(*s, dtype=torch.bfloat16) * 0.2)(F)),
            "w2": _w((lambda *s: torch.randn(*s, dtype=torch.bfloat16) * 0.05)(F, D)),
            "b2": _w((lambda *s: torch.randn(*s, dtype=torch.bfloat16) * 0.8)(D)),
        }
    _B_in = _B_setup()

    return {
        "A_u": _A_in["u"],
        "A_A_re": _A_in["A_re"],
        "A_A_im": _A_in["A_im"],
        "A_B_re": _A_in["B_re"],
        "A_B_im": _A_in["B_im"],
        "A_C_re": _A_in["C_re"],
        "A_C_im": _A_in["C_im"],
        "A_log_dt": _A_in["log_dt"],
        "A_Dskip": _A_in["Dskip"],
        "B_x": _B_in["x"],
        "B_w1": _B_in["w1"],
        "B_b1": _B_in["b1"],
        "B_w2": _B_in["w2"],
        "B_b2": _B_in["b2"],
    }

def reference(A_u, A_A_re, A_A_im, A_B_re, A_B_im, A_C_re, A_C_im, A_log_dt, A_Dskip, B_x, B_w1, B_b1, B_w2, B_b2):
    def _A_ref(u, A_re, A_im, B_re, B_im, C_re, C_im, log_dt, Dskip):
        Bsz, L, D = u.shape
        dt = torch.exp(log_dt)[:, None]
        A = A_re + 1j * A_im
        Bc = B_re + 1j * B_im
        Cc = C_re + 1j * C_im
        logdA = dt * A
        dB = dt * Bc
        l = (lambda x, dt: x.to(dt))(torch.arange(L), A_re.dtype)
        P = torch.exp(logdA[:, :, None] * l[None, None, :])
        K = torch.einsum("dn,dnl,dn->dl", Cc, P, dB).real
        L2 = 2 * L
        Kp = _pad(K, ((0, 0), (0, L)))
        up = (lambda x, *d: x.permute(*d))(u, 0, 2, 1)
        up = _pad(up, ((0, 0), (0, 0), (0, L)))
        Kf = (lambda x, axes=(-2,-1): torch.fft.rfft2(x.float(), dim=axes))(Kp[:, None, :], axes=(-2, -1))
        Uf = (lambda x, axes=(-2,-1): torch.fft.rfft2(x.float(), dim=axes))(up[:, :, None, :], axes=(-2, -1))
        Yf = Uf * Kf[None, :, :, :]
        y = (lambda x, s, axes=(-2,-1): torch.fft.irfft2(x, s=s, dim=axes).to(torch.bfloat16))(Yf, (1, L2), axes=(-2, -1))[:, :, 0, :L]
        y = (lambda x, *d: x.permute(*d))(y, 0, 2, 1) + u * Dskip[None, None, :]
        return y

    def _B_ref(x, w1, b1, w2, b2):
        h = torch.einsum("bsd,df->bsf", x, w1) + b1
        zero = torch.zeros_like(h)
        r = torch.where(h > zero, h, zero)
        h = r * r
        out = torch.einsum("bsf,fd->bsd", h, w2) + b2
        return out

    x = A_u
    xn = _layernorm(x, axis=-1)
    return x + _A_ref(xn, A_A_re, A_A_im, A_B_re, A_B_im, A_C_re, A_C_im, A_log_dt, A_Dskip) + _B_ref(xn, B_w1, B_b1, B_w2, B_b2)
```

```cpp
#include <hip/hip_runtime.h>
#include <hip/hip_bf16.h>
#define PROBE 0
typedef unsigned short u16;
typedef short bf16x8 __attribute__((ext_vector_type(8)));
typedef float f32x4 __attribute__((ext_vector_type(4)));
constexpr int L = 2048, D = 448, N = 256, F = 1792;
__device__ __forceinline__ float b2f(u16 v) { return __uint_as_float((unsigned)v << 16); }
__device__ __forceinline__ u16 f2b(float f) { unsigned u = __float_as_uint(f); u += 0x7fffu + ((u >> 16) & 1u); return (u16)(u >> 16); }
__device__ __forceinline__ float rb(float f) { return b2f(f2b(f)); }

__global__ void k_ln(const u16* x, u16* xn, u16* xnT) {
    int row = blockIdx.x * 4 + (threadIdx.x >> 6), lane = threadIdx.x & 63;
    float v[7], s = 0;
    for (int i = 0; i < 7; ++i) { v[i] = b2f(x[row * D + lane + 64 * i]); s += v[i]; }
    for (int m = 32; m; m >>= 1) s += __shfl_xor(s, m);
    float mean = s / D, q = 0;
    for (int i = 0; i < 7; ++i) q += (v[i] - mean) * (v[i] - mean);
    for (int m = 32; m; m >>= 1) q += __shfl_xor(q, m);
    float mb = rb(mean), sd = rb(sqrtf(rb(rb(q / D) + 1e-5f)));
    for (int i = 0; i < 7; ++i) { u16 o = f2b(rb(v[i] - mb) / sd); xn[row * D + lane + 64 * i] = o; xnT[(lane + 64 * i) * L + row] = o; }
}
__global__ void k_kgen(const u16* Are, const u16* Aim, const u16* Bre, const u16* Bim, const u16* Cre, const u16* Cim, const u16* ldt, float* K) {
    __shared__ float sa[N], sb[N], cr[N], ci[N];
    int d = blockIdx.x, n = threadIdx.x;
    float dt = rb(expf(b2f(ldt[d])));
    { float br = dt * b2f(Bre[d * N + n]), bi = dt * b2f(Bim[d * N + n]), c0 = b2f(Cre[d * N + n]), c1 = b2f(Cim[d * N + n]);
      sa[n] = dt * b2f(Are[d * N + n]) * 1.44269504f; sb[n] = dt * b2f(Aim[d * N + n]) * 0.15915494f; cr[n] = c0 * br - c1 * bi; ci[n] = c0 * bi + c1 * br; }
    __syncthreads();
    int l = blockIdx.y * 256 + threadIdx.x; float lf = rb((float)l), acc = 0;
    for (int j = 0; j < N; ++j) { float e = __builtin_amdgcn_exp2f(sa[j] * lf), ph = __builtin_amdgcn_fractf(sb[j] * lf);
        acc += e * (cr[j] * __builtin_amdgcn_cosf(ph) - ci[j] * __builtin_amdgcn_sinf(ph)); }
    K[d * L + l] = acc;
}
__global__ void k_conv(const float* K, const u16* xnT, float* yT) {
    __shared__ float sk[L], sx[L];
    int d = blockIdx.x;
    for (int i = threadIdx.x; i < L; i += 256) { sk[i] = K[d * L + i]; sx[i] = b2f(xnT[d * L + i]); }
    __syncthreads();
    int t = blockIdx.y * 256 + threadIdx.x; float acc = 0;
    for (int s = 0; s <= t; ++s) acc += sk[t - s] * sx[s];
    yT[d * L + t] = acc;
}
template <int EPI>
__global__ void k_gemm(const u16* A, const u16* B, int Kd, int Nn, const u16* bias, u16* out, const u16* x, const u16* xn, const float* yT, const u16* dsk) {
    int lane = threadIdx.x & 63, tm = blockIdx.y, tn = blockIdx.x * 4 + (threadIdx.x >> 6);
    f32x4 acc = {};
    for (int k0 = 0; k0 < Kd; k0 += 32) {
        bf16x8 a = *(const bf16x8*)(A + (size_t)(tm * 16 + (lane & 15)) * Kd + k0 + 8 * (lane >> 4)), b;
        for (int j = 0; j < 8; ++j) b[j] = (short)B[(size_t)(k0 + 8 * (lane >> 4) + j) * Nn + tn * 16 + (lane & 15)];
        acc = __builtin_amdgcn_mfma_f32_16x16x32_bf16(a, b, acc, 0, 0, 0);
    }
    int col = tn * 16 + (lane & 15);
    for (int j = 0; j < 4; ++j) { int row = tm * 16 + 4 * (lane >> 4) + j; float h = rb(rb(acc[j]) + b2f(bias[col]));
        if (EPI == 0) { h = h > 0 ? h : 0; out[(size_t)row * Nn + col] = f2b(h * h); }
        else { float a = rb(rb(yT[col * L + row]) + rb(b2f(xn[row * D + col]) * b2f(dsk[col]))); out[(size_t)row * Nn + col] = f2b(rb(b2f(x[row * D + col]) + a) + h); } }
}
__global__ void k_probe(const unsigned* p, unsigned* sink, int n, int stride) {
    unsigned o = 0;
    for (int i = 0; i < n; ++i) { unsigned v = p[(size_t)i * stride + (o & 1)]; o = v & 0; asm volatile("" : "+v"(o)); o += (v == 0x9e3779b9u); }
    if (o == 77) sink[0] = o;
}
extern "C" void kernel_launch(void* const* d_in, const int* in_sizes, int n_in, void* d_out, int out_size, void* d_ws, size_t ws_size, hipStream_t stream) {
    const u16* const* in = (const u16* const*)d_in;
    char* ws = (char*)d_ws;
    u16* xn = (u16*)ws; u16* xnT = (u16*)(ws + (2 << 20)); float* K = (float*)(ws + (4 << 20)); float* yT = (float*)(ws + (8 << 20)); u16* H = (u16*)(ws + (12 << 20));
    if (PROBE == 1) k_probe<<<1, 64, 0, stream>>>((const unsigned*)d_in[9], (unsigned*)(ws + (30 << 20)), 400, 1024);
    if (PROBE == 2) k_probe<<<1, 64, 0, stream>>>((const unsigned*)d_in[12], (unsigned*)(ws + (30 << 20)), 390, 1024);
    if (PROBE == 3) k_probe<<<1, 64, 0, stream>>>((const unsigned*)d_in[9], (unsigned*)(ws + (30 << 20)), 0, 1024);
    k_ln<<<L / 4, 256, 0, stream>>>(in[0], xn, xnT);
    k_kgen<<<dim3(D, 8), 256, 0, stream>>>(in[1], in[2], in[3], in[4], in[5], in[6], in[7], K);
    k_conv<<<dim3(D, 8), 256, 0, stream>>>(K, xnT, yT);
    k_gemm<0><<<dim3(F / 64, L / 16), 256, 0, stream>>>(xn, in[10], D, F, in[11], H, nullptr, nullptr, nullptr, nullptr);
    k_gemm<1><<<dim3(D / 64, L / 16), 256, 0, stream>>>(H, in[12], F, D, in[13], (u16*)d_out, in[0], xn, yT, in[8]);
}
```

```cpp
#define L1 163840
#define R1 1
#define G1 256
#define L2 256
#define R2 0
#define G2 256
#define PICK(a,b) ((a) < (b) ? (a) : (b))
#include <hip/hip_runtime.h>
#include <hip/hip_bf16.h>
#define PROBE 0
typedef unsigned short u16;
typedef short bf16x8 __attribute__((ext_vector_type(8)));
typedef float f32x4 __attribute__((ext_vector_type(4)));
constexpr int L = 2048, D = 448, N = 256, F = 1792;
__device__ __forceinline__ float b2f(u16 v) { return __uint_as_float((unsigned)v << 16); }
__device__ __forceinline__ u16 f2b(float f) { unsigned u = __float_as_uint(f); u += 0x7fffu + ((u >> 16) & 1u); return (u16)(u >> 16); }
__device__ __forceinline__ float rb(float f) { return b2f(f2b(f)); }

__global__ void k_ln(const u16* x, u16* xn, u16* xnT) {
    int row = blockIdx.x * 4 + (threadIdx.x >> 6), lane = threadIdx.x & 63;
    float v[7], s = 0;
    for (int i = 0; i < 7; ++i) { v[i] = b2f(x[row * D + lane + 64 * i]); s += v[i]; }
    for (int m = 32; m; m >>= 1) s += __shfl_xor(s, m);
    float mean = s / D, q = 0;
    for (int i = 0; i < 7; ++i) q += (v[i] - mean) * (v[i] - mean);
    for (int m = 32; m; m >>= 1) q += __shfl_xor(q, m);
    float mb = rb(mean), sd = rb(sqrtf(rb(rb(q / D) + 1e-5f)));
    for (int i = 0; i < 7; ++i) { u16 o = f2b(rb(v[i] - mb) / sd); xn[row * D + lane + 64 * i] = o; xnT[(lane + 64 * i) * L + row] = o; }
}
__global__ void k_kgen(const u16* Are, const u16* Aim, const u16* Bre, const u16* Bim, const u16* Cre, const u16* Cim, const u16* ldt, float* K) {
    __shared__ float sa[N], sb[N], cr[N], ci[N];
    int d = blockIdx.x, n = threadIdx.x;
    float dt = rb(expf(b2f(ldt[d])));
    { float br = dt * b2f(Bre[d * N + n]), bi = dt * b2f(Bim[d * N + n]), c0 = b2f(Cre[d * N + n]), c1 = b2f(Cim[d * N + n]);
      sa[n] = dt * b2f(Are[d * N + n]) * 1.44269504f; sb[n] = dt * b2f(Aim[d * N + n]) * 0.15915494f; cr[n] = c0 * br - c1 * bi; ci[n] = c0 * bi + c1 * br; }
    __syncthreads();
    int l = blockIdx.y * 256 + threadIdx.x; float lf = rb((float)l), acc = 0;
    for (int j = 0; j < N; ++j) { float e = __builtin_amdgcn_exp2f(sa[j] * lf), ph = __builtin_amdgcn_fractf(sb[j] * lf);
        acc += e * (cr[j] * __builtin_amdgcn_cosf(ph) - ci[j] * __builtin_amdgcn_sinf(ph)); }
    K[d * L + l] = acc;
}
__global__ void k_conv(const float* K, const u16* xnT, float* yT) {
    __shared__ float sk[L], sx[L];
    int d = blockIdx.x;
    for (int i = threadIdx.x; i < L; i += 256) { sk[i] = K[d * L + i]; sx[i] = b2f(xnT[d * L + i]); }
    __syncthreads();
    int t = blockIdx.y * 256 + threadIdx.x; float acc = 0;
    for (int s = 0; s <= t; ++s) acc += sk[t - s] * sx[s];
    yT[d * L + t] = acc;
}
template <int EPI>
__global__ void k_gemm(const u16* A, const u16* B, int Kd, int Nn, const u16* bias, u16* out, const u16* x, const u16* xn, const float* yT, const u16* dsk) {
    int lane = threadIdx.x & 63, tm = blockIdx.y, tn = blockIdx.x * 4 + (threadIdx.x >> 6);
    f32x4 acc = {};
    for (int k0 = 0; k0 < Kd; k0 += 32) {
        bf16x8 a = *(const bf16x8*)(A + (size_t)(tm * 16 + (lane & 15)) * Kd + k0 + 8 * (lane >> 4)), b;
        for (int j = 0; j < 8; ++j) b[j] = (short)B[(size_t)(k0 + 8 * (lane >> 4) + j) * Nn + tn * 16 + (lane & 15)];
        acc = __builtin_amdgcn_mfma_f32_16x16x32_bf16(a, b, acc, 0, 0, 0);
    }
    int col = tn * 16 + (lane & 15);
    for (int j = 0; j < 4; ++j) { int row = tm * 16 + 4 * (lane >> 4) + j; float h = rb(rb(acc[j]) + b2f(bias[col]));
        if (EPI == 0) { h = h > 0 ? h : 0; out[(size_t)row * Nn + col] = f2b(h * h); }
        else { float a = rb(rb(yT[col * L + row]) + rb(b2f(xn[row * D + col]) * b2f(dsk[col]))); out[(size_t)row * Nn + col] = f2b(rb(b2f(x[row * D + col]) + a) + h); } }
}
__global__ void k_probe(const unsigned* p, unsigned* sink, int n, int stride) {
    unsigned o = 0;
    for (int i = 0; i < n; ++i) { unsigned v = p[(size_t)i * stride + (o & 1)]; o = v & 0; asm volatile("" : "+v"(o)); o += (v == 0x9e3779b9u); }
    if (o == 77) sink[0] = o;
}

template <int LDS, int REGS>
__global__ void __launch_bounds__(256, 1) k_mark(unsigned long long* t) {
    __shared__ char lds[LDS];
    unsigned long long t0 = __builtin_amdgcn_s_memrealtime();
    if (REGS) asm volatile("v_mov_b32 v255, 0\n v_accvgpr_write_b32 a255, v255" ::: "v255", "a255");
    lds[threadIdx.x] = (char)t0;
    __syncthreads();
    if (threadIdx.x == 0) t[blockIdx.x * 16] = t0 + (lds[1] != lds[2]);
}
template <int LDS, int REGS>
__global__ void __launch_bounds__(256, 1) k_until(const unsigned long long* t, int n) {
    __shared__ char lds[LDS];
    if (REGS) asm volatile("v_mov_b32 v255, 0\n v_accvgpr_write_b32 a255, v255" ::: "v255", "a255");
    unsigned long long t0 = t[(threadIdx.x % n) * 16];
    for (int m = 32; m; m >>= 1) { unsigned long long o = __shfl_xor(t0, m); t0 = PICK(o, t0); }
    lds[threadIdx.x] = 0;
    __syncthreads();
    for (int i = 0; i < 2000000 && __builtin_amdgcn_s_memrealtime() - t0 + lds[3] < 40000ull; ++i) __builtin_amdgcn_s_sleep(1);
}
extern "C" void kernel_launch(void* const* d_in, const int* in_sizes, int n_in, void* d_out, int out_size, void* d_ws, size_t ws_size, hipStream_t stream) {
    const u16* const* in = (const u16* const*)d_in;
    char* ws = (char*)d_ws;
    u16* xn = (u16*)ws; u16* xnT = (u16*)(ws + (2 << 20)); float* K = (float*)(ws + (4 << 20)); float* yT = (float*)(ws + (8 << 20)); u16* H = (u16*)(ws + (12 << 20));
    if (PROBE == 1) k_probe<<<1, 64, 0, stream>>>((const unsigned*)d_in[9], (unsigned*)(ws + (30 << 20)), 400, 1024);
    if (PROBE == 2) k_probe<<<1, 64, 0, stream>>>((const unsigned*)d_in[12], (unsigned*)(ws + (30 << 20)), 390, 1024);
    if (PROBE == 3) k_probe<<<1, 64, 0, stream>>>((const unsigned*)d_in[9], (unsigned*)(ws + (30 << 20)), 0, 1024);
    k_mark<L1, R1><<<G1, 256, 0, stream>>>((unsigned long long*)(ws + (31 << 20)));
    k_ln<<<L / 4, 256, 0, stream>>>(in[0], xn, xnT);
    k_kgen<<<dim3(D, 8), 256, 0, stream>>>(in[1], in[2], in[3], in[4], in[5], in[6], in[7], K);
    k_conv<<<dim3(D, 8), 256, 0, stream>>>(K, xnT, yT);
    k_gemm<0><<<dim3(F / 64, L / 16), 256, 0, stream>>>(xn, in[10], D, F, in[11], H, nullptr, nullptr, nullptr, nullptr);
    k_gemm<1><<<dim3(D / 64, L / 16), 256, 0, stream>>>(H, in[12], F, D, in[13], (u16*)d_out, in[0], xn, yT, in[8]);
    k_until<L2, R2><<<G2, 256, 0, stream>>>((unsigned long long*)(ws + (31 << 20)), G1 < 64 ? G1 : 64);
}
```

```cpp
#include <hip/hip_runtime.h>

constexpr unsigned WS_BYTES = 13959168;

struct Buf { const void* p; unsigned bytes; unsigned word3; };
struct Args { Buf b[15]; };

__global__ void __launch_bounds__(512, 2) fused_s4_mlp(Args a) {
    __shared__ __attribute__((aligned(16))) char lds[163840];
    asm volatile(
    "s_load_dwordx16 s[8:23], %0, 0x0\n"
    "s_load_dwordx16 s[24:39], %0, 0x40\n"
    "s_load_dwordx16 s[40:55], %0, 0x80\n"
    "s_load_dwordx8 s[56:63], %0, 0xc0\n"
    "s_load_dwordx4 s[64:67], %0, 0xe0\n"
    "v_lshrrev_b32 v3, 6, v0\n"
    "v_and_b32 v1, 63, v0\n"
    "v_readfirstlane_b32 s68, v3\n"
    "s_and_b32 s70, %1, 7\n"
    "s_lshr_b32 s71, %1, 3\n"
    "v_lshlrev_b32 v2, 4, v1\n"
    "s_lshl_b32 s74, s71, 3\n"
    "s_add_u32 s74, s74, s68\n"
    "s_getpc_b64 s[96:97]\n"
    ".Lpcm%=:\n"
    "s_add_u32 s96, s96, .Lmain%=-.Lpcm%=\n"
    "s_addc_u32 s97, s97, 0\n"
    "s_mov_b32 s98, .Lend%=-.Lmain%=\n"
    "s_mov_b32 s99, 0x20000\n"
    "s_lshl_b32 s74, s74, 7\n"
    "s_mov_b64 exec, 1\n"
    "buffer_load_dword v7, off, s[96:99], s74\n"
    "s_mov_b64 exec, -1\n"
    "s_cmp_lt_u32 s71, 28\n"
    "s_cbranch_scc1 .Lmain%=\n"
    "s_sub_u32 s71, s71, 28\n"
    "s_lshl_b32 s79, s70, 2\n"
    "s_add_u32 s79, s79, s71\n"
    "s_lshl_b32 s74, s79, 3\n"
    "s_add_u32 s74, s74, s68\n"
    "s_mul_i32 s82, s74, 0x1c00\n"
    "s_mul_i32 s81, s68, 0x1c10\n"
    "s_mov_b32 m0, s81\n"
    "s_waitcnt lgkmcnt(0)\n"
    "buffer_load_dwordx4 v2, s[40:43], s82 offen lds\n"
    "buffer_load_dwordx4 v2, s[40:43], s82 offen offset:1024 lds\n"
    "buffer_load_dwordx4 v2, s[40:43], s82 offen offset:2048 lds\n"
    "buffer_load_dwordx4 v2, s[40:43], s82 offen offset:3072 lds\n"
    "s_add_u32 m0, s81, 0x1000\n"
    "s_add_u32 s82, s82, 0x1000\n"
    "buffer_load_dwordx4 v2, s[40:43], s82 offen lds\n"
    "buffer_load_dwordx4 v2, s[40:43], s82 offen offset:1024 lds\n"
    "buffer_load_dwordx4 v2, s[40:43], s82 offen offset:2048 lds\n"
    "s_buffer_load_dwordx2 s[76:77], s[52:55], 0x0\n"
    "s_branch .Lhelper%=\n"
    ".Lmain%=:\n"
    "s_mov_b32 s82, %1\n"
    "v_and_b32 v16, 0xff, v0\n"
    "s_lshl_b32 s90, s82, 10\n"
    "s_lshl_b32 s91, s82, 2\n"
    "v_lshlrev_b32 v16, 2, v16\n"
    "s_and_b32 s69, s68, 3\n"
    "s_and_b32 s72, s71, 3\n"
    "s_lshr_b32 s79, s71, 2\n"
    "s_and_b32 s74, s70, 6\n"
    "s_lshl1_add_u32 s80, s74, s72\n"
    "s_and_b32 s81, s70, 1\n"
    "s_lshl_b32 s89, s69, 12\n"
    "v_lshrrev_b32 v17, 5, v1\n"
    "s_cmp_ge_u32 s68, 4\n"
    "s_cbranch_scc1 .Lssm%=\n"
    ".Lcut1%=:\n"
    "s_lshl1_add_u32 s83, s72, s79\n"
    "s_bfe_u32 s74, s70, 0x10001\n"
    "s_add_u32 s83, s83, s74\n"
    "s_sub_u32 s74, s83, 7\n"
    "s_min_u32 s83, s83, s74\n"
    "v_lshrrev_b32 v3, 3, v1\n"
    "v_lshrrev_b32 v4, 4, v1\n"
    "v_and_b32 v5, 7, v1\n"
    "s_xnor_b32 s74, s69, s81\n"
    "s_and_b32 s74, s74, 1\n"
    "s_lshl1_add_u32 s74, s80, s74\n"
    "v_xor_b32 v6, v5, v4\n"
    "v_lshlrev_b32 v6, 4, v6\n"
    "v_mul_u32_u24 v40, 0x700, v3\n"
    "s_lshl_b32 s74, s74, 6\n"
    "s_lshr_b32 s75, s69, 1\n"
    "s_add_u32 s74, s74, s75\n"
    "s_mul_i32 s86, s74, 0x380\n"
    "v_add_u32 v40, v40, v6\n"
    "v_xor_b32 v43, 64, v40\n"
    "v_add_u32 v41, 0x3400, v43\n"
    "v_add_u32 v42, 0x6800, v40\n"
    "v_add_u32 v43, 0x9c00, v43\n"
    "s_lshl_b32 s84, s83, 7\n"
    "s_add_u32 m0, s89, 0\n"
    "s_add_u32 s84, s84, s86\n"
    "s_waitcnt lgkmcnt(0)\n"
    "s_buffer_load_dword s92, s[32:35], s91\n"
    "buffer_load_dword v10, v16, s[8:11], s90 offen\n"
    "buffer_load_dword v11, v16, s[12:15], s90 offen\n"
    "s_buffer_load_dwordx2 s[76:77], s[52:55], 0x0\n"
    "v_and_b32 v6, 15, v1\n"
    "v_lshlrev_b32 v45, 2, v4\n"
    "v_xor_b32 v6, v6, v45\n"
    "v_lshlrev_b32 v6, 4, v6\n"
    "s_mul_i32 s74, s69, 0xe000\n"
    "s_mul_i32 s75, s81, 7\n"
    "s_add_u32 s75, s75, s79\n"
    "s_lshl_b32 s75, s75, 8\n"
    "s_add_u32 s87, s74, s75\n"
    "v_mul_u32_u24 v44, 0xe00, v4\n"
    "s_barrier\n"
    "buffer_load_dwordx4 v40, s[40:43], s84 offen lds\n"
    "buffer_load_dwordx4 v41, s[40:43], s84 offen offset:1024 lds\n"
    "buffer_load_dwordx4 v42, s[40:43], s84 offen offset:2048 lds\n"
    "buffer_load_dwordx4 v43, s[40:43], s84 offen offset:3072 lds\n"
    "v_add_u32 v44, v44, v6\n"
    "v_xor_b32 v47, 32, v44\n"
    "v_add_u32 v45, 0x3400, v44\n"
    "v_add_u32 v46, 0x6800, v47\n"
    "v_add_u32 v47, 0x9c00, v47\n"
    "s_mul_i32 s85, s83, 0x38000\n"
    "s_add_u32 m0, s89, 0x4000\n"
    "s_add_u32 s85, s85, s87\n"
    "buffer_load_dwordx4 v44, s[44:47], s85 offen lds\n"
    "buffer_load_dwordx4 v45, s[44:47], s85 offen offset:1024 lds\n"
    "s_add_u32 s83, s83, 1\n"
    "s_cmp_eq_u32 s83, 7\n"
    "s_cselect_b32 s83, 0, s83\n"
    "buffer_load_dwordx4 v46, s[44:47], s85 offen offset:2048 lds\n"
    "buffer_load_dwordx4 v47, s[44:47], s85 offen offset:3072 lds\n"
    "s_lshl1_add_u32 s74, s89, 0x20000\n"
    "v_lshl_add_u32 v18, v1, 3, s74\n"
    "v_xor_b32 v19, 32, v18\n"
    "v_xor_b32 v20, 64, v18\n"
    "v_xor_b32 v21, 0x60, v18\n"
    "v_xor_b32 v22, 0x80, v18\n"
    "v_xor_b32 v23, 0xa0, v18\n"
    "v_xor_b32 v24, 0xc0, v18\n"
    "v_xor_b32 v25, 0xe0, v18\n"
    "v_and_b32 v68, 31, v1\n"
    "v_lshrrev_b32 v5, 4, v1\n"
    "v_and_b32 v4, 15, v1\n"
    "v_bfe_u32 v3, v1, 1, 3\n"
    "v_xor_b32 v3, v3, v5\n"
    "v_lshlrev_b32 v3, 4, v3\n"
    "v_lshl_add_u32 v3, v4, 7, v3\n"
    "s_and_b32 s75, s69, 1\n"
    "s_mul_i32 s74, s75, 0x2040\n"
    "v_xor_b32 v48, s74, v3\n"
    "s_lshl_b32 s75, s75, 13\n"
    "v_xor_b32 v49, 64, v48\n"
    "v_bfe_u32 v69, v1, 2, 2\n"
    "v_bfe_u32 v3, v1, 4, 1\n"
    "v_and_b32 v4, 3, v1\n"
    "v_lshlrev_b32 v4, 3, v4\n"
    "v_lshl_or_b32 v70, v3, 5, v4\n"
    "s_lshr_b32 s74, s69, 1\n"
    "s_lshl_b32 s74, s74, 7\n"
    "v_lshlrev_b32 v3, 6, v69\n"
    "v_xor_b32 v3, s74, v3\n"
    "v_or_b32 v3, v3, v70\n"
    "v_lshl_add_u32 v4, v5, 3, v69\n"
    "v_lshl_add_u32 v3, v4, 8, v3\n"
    "v_add_u32 v3, 0x4000, v3\n"
    "v_add_u32 v52, s75, v3\n"
    "v_subrev_u32 v56, s75, v3\n"
    "v_xor_b32 v53, 32, v52\n"
    "v_xor_b32 v54, 64, v52\n"
    "v_xor_b32 v55, 0x60, v52\n"
    "v_xor_b32 v57, 32, v56\n"
    "v_xor_b32 v58, 64, v56\n"
    "v_xor_b32 v59, 0x60, v56\n"
    "v_mov_b32 v4, 0x3f803f80\n"
    "v_cmp_eq_u32 vcc, 0, v69\n"
    "s_nop 1\n"
    "v_cndmask_b32 v72, 0, v4, vcc\n"
    "v_mov_b32 v73, v72\n"
    "v_mov_b32 v74, v72\n"
    "v_mov_b32 v75, v72\n"
    "v_cmp_eq_u32 vcc, 1, v69\n"
    "s_nop 1\n"
    "v_cndmask_b32 v76, 0, v4, vcc\n"
    "v_mov_b32 v77, v76\n"
    "v_mov_b32 v78, v76\n"
    "v_mov_b32 v79, v76\n"
    "v_cmp_eq_u32 vcc, 2, v69\n"
    "s_nop 1\n"
    "v_cndmask_b32 v80, 0, v4, vcc\n"
    "v_mov_b32 v81, v80\n"
    "v_mov_b32 v82, v80\n"
    "v_mov_b32 v83, v80\n"
    "v_cmp_eq_u32 vcc, 3, v69\n"
    "s_nop 1\n"
    "v_cndmask_b32 v84, 0, v4, vcc\n"
    "v_mov_b32 v85, v84\n"
    "v_mov_b32 v86, v84\n"
    "v_mov_b32 v87, v84\n"
    "v_mov_b32 v38, 0x3f803f80\n"
    "v_mov_b32 v39, 0\n"
    "ds_write_b64 v18, v[38:39]\n"
    "s_lshr_b32 s74, s69, 1\n"
    "s_lshl_b32 s74, s74, 4\n"
    "s_waitcnt lgkmcnt(0)\n"
    "s_lshr_b32 s92, s92, s74\n"
    "s_lshl_b32 s92, s92, 16\n"
    "v_mov_b32 v6, s92\n"
    "v_mul_f32 v3, 0x3fb8aa3b, v6\n"
    "v_mov_b32 v5, 0x3fb8aa3b\n"
    "v_fma_f32 v4, v6, v5, -v3\n"
    "v_exp_f32 v3, v3\n"
    "v_fmac_f32 v4, 0x32a57060, v6\n"
    "v_mul_f32 v4, 0x3f317218, v4\n"
    "v_fmac_f32 v3, v3, v4\n"
    "v_cvt_pk_bf16_f32 v3, v3, v3\n"
    "v_lshlrev_b32 v26, 16, v3\n"
    "v_lshlrev_b32 v27, 16, v3\n"
    "v_mul_f32 v3, 0x3fb8aa3b, v26\n"
    "v_mul_f32 v4, 0x3e22f983, v26\n"
    "s_add_u32 s76, s76, 1\n"
    "s_addc_u32 s77, s77, 0\n"
    "v_mov_b32 v8, s76\n"
    "v_mov_b32 v9, s77\n"
    "s_waitcnt vmcnt(9)\n"
    "v_lshlrev_b32 v28, 16, v10\n"
    "v_and_b32 v29, 0xffff0000, v10\n"
    "s_waitcnt vmcnt(8)\n"
    "v_lshlrev_b32 v30, 16, v11\n"
    "v_and_b32 v31, 0xffff0000, v11\n"
    "v_mul_f32 v28, v3, v28\n"
    "v_mul_f32 v29, v3, v29\n"
    "v_mul_f32 v30, v4, v30\n"
    "v_mul_f32 v31, v4, v31\n"
    "v_mul_f32 v32, 0x41000000, v28\n"
    "v_mul_f32 v34, 0x41000000, v30\n"
    "v_exp_f32 v32, v32\n"
    "v_fract_f32 v34, v34\n"
    "v_mul_f32 v33, 0x41000000, v29\n"
    "v_mul_f32 v35, 0x41000000, v31\n"
    "v_exp_f32 v33, v33\n"
    "v_fract_f32 v35, v35\n"
    "v_cos_f32 v88, v34\n"
    "v_sin_f32 v90, v34\n"
    "v_cos_f32 v89, v35\n"
    "v_sin_f32 v91, v35\n"
    "v_pk_mul_f32 v[88:89], v[88:89], v[32:33]\n"
    "v_pk_mul_f32 v[90:91], v[90:91], v[32:33]\n"
    "v_cvt_pk_bf16_f32 v36, v88, v89\n"
    "v_cvt_pk_bf16_f32 v37, v90, v91\n"
    "ds_write_b64 v19, v[36:37] offset:512\n"
    "v_pk_mul_f32 v[92:93], v[88:89], v[88:89]\n"
    "v_pk_mul_f32 v[94:95], v[90:91], v[88:89]\n"
    "v_pk_fma_f32 v[92:93], v[90:91], v[90:91], v[92:93] neg_lo:[1,0,0] neg_hi:[1,0,0]\n"
    "v_pk_fma_f32 v[94:95], v[88:89], v[90:91], v[94:95]\n"
    "v_cvt_pk_bf16_f32 v38, v92, v93\n"
    "v_cvt_pk_bf16_f32 v39, v94, v95\n"
    "ds_write_b64 v20, v[38:39] offset:1024\n"
    "v_pk_mul_f32 v[96:97], v[92:93], v[88:89]\n"
    "v_pk_mul_f32 v[98:99], v[94:95], v[88:89]\n"
    "v_pk_fma_f32 v[96:97], v[94:95], v[90:91], v[96:97] neg_lo:[1,0,0] neg_hi:[1,0,0]\n"
    "v_pk_fma_f32 v[98:99], v[92:93], v[90:91], v[98:99]\n"
    "v_cvt_pk_bf16_f32 v36, v96, v97\n"
    "v_cvt_pk_bf16_f32 v37, v98, v99\n"
    "ds_write_b64 v21, v[36:37] offset:1536\n"
    "v_pk_mul_f32 v[92:93], v[96:97], v[88:89]\n"
    "v_pk_mul_f32 v[94:95], v[98:99], v[88:89]\n"
    "v_pk_fma_f32 v[92:93], v[98:99], v[90:91], v[92:93] neg_lo:[1,0,0] neg_hi:[1,0,0]\n"
    "v_pk_fma_f32 v[94:95], v[96:97], v[90:91], v[94:95]\n"
    "v_cvt_pk_bf16_f32 v38, v92, v93\n"
    "v_cvt_pk_bf16_f32 v39, v94, v95\n"
    "ds_write_b64 v22, v[38:39] offset:2048\n"
    "v_pk_mul_f32 v[96:97], v[92:93], v[88:89]\n"
    "v_pk_mul_f32 v[98:99], v[94:95], v[88:89]\n"
    "v_pk_fma_f32 v[96:97], v[94:95], v[90:91], v[96:97] neg_lo:[1,0,0] neg_hi:[1,0,0]\n"
    "v_pk_fma_f32 v[98:99], v[92:93], v[90:91], v[98:99]\n"
    "v_cvt_pk_bf16_f32 v36, v96, v97\n"
    "v_cvt_pk_bf16_f32 v37, v98, v99\n"
    "ds_write_b64 v23, v[36:37] offset:2560\n"
    "v_pk_mul_f32 v[92:93], v[96:97], v[88:89]\n"
    "v_pk_mul_f32 v[94:95], v[98:99], v[88:89]\n"
    "v_pk_fma_f32 v[92:93], v[98:99], v[90:91], v[92:93] neg_lo:[1,0,0] neg_hi:[1,0,0]\n"
    "v_pk_fma_f32 v[94:95], v[96:97], v[90:91], v[94:95]\n"
    "v_cvt_pk_bf16_f32 v38, v92, v93\n"
    "v_cvt_pk_bf16_f32 v39, v94, v95\n"
    "ds_write_b64 v24, v[38:39] offset:3072\n"
    "v_pk_mul_f32 v[96:97], v[92:93], v[88:89]\n"
    "v_pk_mul_f32 v[98:99], v[94:95], v[88:89]\n"
    "v_pk_fma_f32 v[96:97], v[94:95], v[90:91], v[96:97] neg_lo:[1,0,0] neg_hi:[1,0,0]\n"
    "v_pk_fma_f32 v[98:99], v[92:93], v[90:91], v[98:99]\n"
    "v_cvt_pk_bf16_f32 v36, v96, v97\n"
    "v_cvt_pk_bf16_f32 v37, v98, v99\n"
    "ds_write_b64 v25, v[36:37] offset:3584\n"
    "v_pk_mul_f32 v[92:93], v[96:97], v[88:89]\n"
    "v_pk_mul_f32 v[94:95], v[98:99], v[88:89]\n"
    "v_pk_fma_f32 v[92:93], v[98:99], v[90:91], v[92:93] neg_lo:[1,0,0] neg_hi:[1,0,0]\n"
    "v_pk_fma_f32 v[94:95], v[96:97], v[90:91], v[94:95]\n"
    "v_cvt_pk_bf16_f32 v38, v92, v93\n"
    "v_cvt_pk_bf16_f32 v39, v94, v95\n"
    "ds_write_b64 v18, v[38:39] offset:4096\n"
    "v_pk_mul_f32 v[96:97], v[92:93], v[88:89]\n"
    "v_pk_mul_f32 v[98:99], v[94:95], v[88:89]\n"
    "v_pk_fma_f32 v[96:97], v[94:95], v[90:91], v[96:97] neg_lo:[1,0,0] neg_hi:[1,0,0]\n"
    "v_pk_fma_f32 v[98:99], v[92:93], v[90:91], v[98:99]\n"
    "v_cvt_pk_bf16_f32 v36, v96, v97\n"
    "v_cvt_pk_bf16_f32 v37, v98, v99\n"
    "ds_write_b64 v19, v[36:37] offset:4608\n"
    "v_pk_mul_f32 v[92:93], v[96:97], v[88:89]\n"
    "v_pk_mul_f32 v[94:95], v[98:99], v[88:89]\n"
    "v_pk_fma_f32 v[92:93], v[98:99], v[90:91], v[92:93] neg_lo:[1,0,0] neg_hi:[1,0,0]\n"
    "v_pk_fma_f32 v[94:95], v[96:97], v[90:91], v[94:95]\n"
    "v_cvt_pk_bf16_f32 v38, v92, v93\n"
    "v_cvt_pk_bf16_f32 v39, v94, v95\n"
    "ds_write_b64 v20, v[38:39] offset:5120\n"
    "v_pk_mul_f32 v[96:97], v[92:93], v[88:89]\n"
    "v_pk_mul_f32 v[98:99], v[94:95], v[88:89]\n"
    "v_pk_fma_f32 v[96:97], v[94:95], v[90:91], v[96:97] neg_lo:[1,0,0] neg_hi:[1,0,0]\n"
    "v_pk_fma_f32 v[98:99], v[92:93], v[90:91], v[98:99]\n"
    "v_cvt_pk_bf16_f32 v36, v96, v97\n"
    "v_cvt_pk_bf16_f32 v37, v98, v99\n"
    "ds_write_b64 v21, v[36:37] offset:5632\n"
    "v_pk_mul_f32 v[92:93], v[96:97], v[88:89]\n"
    "v_pk_mul_f32 v[94:95], v[98:99], v[88:89]\n"
    "v_pk_fma_f32 v[92:93], v[98:99], v[90:91], v[92:93] neg_lo:[1,0,0] neg_hi:[1,0,0]\n"
    "v_pk_fma_f32 v[94:95], v[96:97], v[90:91], v[94:95]\n"
    "v_cvt_pk_bf16_f32 v38, v92, v93\n"
    "v_cvt_pk_bf16_f32 v39, v94, v95\n"
    "ds_write_b64 v22, v[38:39] offset:6144\n"
    "v_pk_mul_f32 v[96:97], v[92:93], v[88:89]\n"
    "v_pk_mul_f32 v[98:99], v[94:95], v[88:89]\n"
    "v_pk_fma_f32 v[96:97], v[94:95], v[90:91], v[96:97] neg_lo:[1,0,0] neg_hi:[1,0,0]\n"
    "v_pk_fma_f32 v[98:99], v[92:93], v[90:91], v[98:99]\n"
    "v_cvt_pk_bf16_f32 v36, v96, v97\n"
    "v_cvt_pk_bf16_f32 v37, v98, v99\n"
    "ds_write_b64 v23, v[36:37] offset:6656\n"
    "v_pk_mul_f32 v[92:93], v[96:97], v[88:89]\n"
    "v_pk_mul_f32 v[94:95], v[98:99], v[88:89]\n"
    "v_pk_fma_f32 v[92:93], v[98:99], v[90:91], v[92:93] neg_lo:[1,0,0] neg_hi:[1,0,0]\n"
    "v_pk_fma_f32 v[94:95], v[96:97], v[90:91], v[94:95]\n"
    "v_cvt_pk_bf16_f32 v38, v92, v93\n"
    "v_cvt_pk_bf16_f32 v39, v94, v95\n"
    "ds_write_b64 v24, v[38:39] offset:7168\n"
    "v_pk_mul_f32 v[96:97], v[92:93], v[88:89]\n"
    "v_pk_mul_f32 v[98:99], v[94:95], v[88:89]\n"
    "v_pk_fma_f32 v[96:97], v[94:95], v[90:91], v[96:97] neg_lo:[1,0,0] neg_hi:[1,0,0]\n"
    "v_pk_fma_f32 v[98:99], v[92:93], v[90:91], v[98:99]\n"
    "v_cvt_pk_bf16_f32 v36, v96, v97\n"
    "v_cvt_pk_bf16_f32 v37, v98, v99\n"
    "ds_write_b64 v25, v[36:37] offset:7680\n"
    "s_lshl_b32 s84, s83, 7\n"
    "s_add_u32 m0, s89, 0x8000\n"
    "s_add_u32 s84, s84, s86\n"
    "buffer_load_dwordx4 v40, s[40:43], s84 offen lds\n"
    "buffer_load_dwordx4 v41, s[40:43], s84 offen offset:1024 lds\n"
    "buffer_load_dwordx4 v42, s[40:43], s84 offen offset:2048 lds\n"
    "buffer_load_dwordx4 v43, s[40:43], s84 offen offset:3072 lds\n"
    "s_mul_i32 s85, s83, 0x38000\n"
    "s_add_u32 m0, s89, 0xc000\n"
    "s_add_u32 s85, s85, s87\n"
    "buffer_load_dwordx4 v44, s[44:47], s85 offen lds\n"
    "buffer_load_dwordx4 v45, s[44:47], s85 offen offset:1024 lds\n"
    "s_add_u32 s83, s83, 1\n"
    "s_cmp_eq_u32 s83, 7\n"
    "s_cselect_b32 s83, 0, s83\n"
    "buffer_load_dwordx4 v46, s[44:47], s85 offen offset:2048 lds\n"
    "buffer_load_dwordx4 v47, s[44:47], s85 offen offset:3072 lds\n"
    "s_setprio 2\n"
    "v_add_u32 v50, 0x10000, v48\n"
    "v_add_u32 v51, 0x10000, v49\n"
    "v_add_u32 v60, 0x10000, v52\n"
    "v_add_u32 v61, 0x10000, v53\n"
    "v_add_u32 v62, 0x10000, v54\n"
    "v_add_u32 v63, 0x10000, v55\n"
    "v_add_u32 v64, 0x10000, v56\n"
    "v_add_u32 v65, 0x10000, v57\n"
    "v_add_u32 v66, 0x10000, v58\n"
    "v_add_u32 v67, 0x10000, v59\n"
    "v_lshrrev_b32 v3, 3, v1\n"
    "v_bfe_u32 v6, v1, 4, 1\n"
    "v_lshlrev_b32 v6, 2, v6\n"
    "v_and_b32 v5, 7, v1\n"
    "v_xor_b32 v6, v6, v5\n"
    "v_lshlrev_b32 v6, 4, v6\n"
    "v_mul_u32_u24 v192, 0x380, v3\n"
    "v_add_u32 v192, v192, v6\n"
    "v_add_u32 v193, 0x1800, v192\n"
    "v_add_u32 v194, 0x3000, v192\n"
    "v_add_u32 v195, 0x4800, v192\n"
    "s_mul_i32 s74, s81, 28\n"
    "s_add_u32 s74, s74, s69\n"
    "s_mul_i32 s74, s74, 0x7000\n"
    "s_lshl_b32 s75, s79, 7\n"
    "s_add_u32 s92, s74, s75\n"
    "s_mov_b32 s93, s79\n"
    "v_lshrrev_b32 v3, 1, v69\n"
    "v_lshlrev_b32 v3, 6, v3\n"
    "v_or_b32 v3, v3, v70\n"
    "v_lshl_add_u32 v4, v17, 3, v69\n"
    "v_lshl_add_u32 v3, v4, 7, v3\n"
    "s_lshr_b32 s74, s69, 1\n"
    "s_lshl_b32 s74, s74, 13\n"
    "v_add_u32 v216, s74, v3\n"
    "v_xor_b32 v217, 64, v216\n"
    "v_add_u32 v218, 0x10000, v216\n"
    "v_add_u32 v219, 0x10000, v217\n"
    "v_and_b32 v38, 15, v1\n"
    "v_lshlrev_b32 v38, 3, v38\n"
    "s_lshl1_add_u32 s74, s80, s81\n"
    "s_and_b32 s75, s69, 1\n"
    "s_lshl1_add_u32 s74, s74, s75\n"
    "s_lshl_b32 s74, s74, 8\n"
    "s_add_u32 s73, s74, 0x8000\n"
    "s_xor_b32 s72, s73, 0x200\n"
    "s_mul_i32 s74, s81, 7\n"
    "s_add_u32 s74, s74, s79\n"
    "s_lshr_b32 s75, s69, 1\n"
    "s_lshl1_add_u32 s74, s74, s75\n"
    "s_lshl_b32 s74, s74, 7\n"
    "s_lshl_b32 s75, s69, 9\n"
    "s_add_u32 s75, s75, 0x27400\n"
    "v_lshl_add_u32 v15, v1, 2, s75\n"
    "v_mov_b32 v16, 0\n"
    "v_lshlrev_b32 v3, 1, v1\n"
    "buffer_load_short_d16_hi v16, v3, s[60:63], s74 offen\n"
    "s_lshl_b32 s74, s80, 1\n"
    "s_add_u32 s74, s74, s81\n"
    "s_lshl_b32 s75, s74, 2\n"
    "s_add_u32 s75, s75, s69\n"
    "s_mul_i32 s75, s75, 0xe000\n"
    "s_add_u32 s88, s75, 0x5d0000\n"
    "s_lshl_b32 s74, s74, 3\n"
    "s_add_u32 s74, s74, s69\n"
    "s_lshl_b32 s74, s74, 10\n"
    "s_add_u32 s70, s74, 0x10000\n"
    "s_lshl_b32 s75, s79, 7\n"
    "s_add_u32 s75, s75, s70\n"
    "v_and_b32 v3, 0x70, v2\n"
    "v_add_u32 v4, s75, v3\n"
    "s_lshr_b32 s74, s69, 1\n"
    "s_lshl1_add_u32 s74, s82, s74\n"
    "s_and_b32 s75, s69, 1\n"
    "s_lshl_b32 s75, s75, 9\n"
    "s_add_u32 s74, s74, s75\n"
    "s_lshl_b32 s74, s74, 7\n"
    "s_add_u32 s74, s74, 0x50000\n"
    "v_add_u32 v5, s74, v3\n"
    "v_cmp_lt_u32 vcc, 7, v1\n"
    "s_nop 1\n"
    "v_cndmask_b32 v4, v4, v5, vcc\n"
    "s_mul_i32 s74, s80, 7\n"
    "s_add_u32 s74, s74, s79\n"
    "s_lshl1_add_u32 s74, s74, s81\n"
    "s_lshl2_add_u32 s74, s74, s69\n"
    "s_xor_b32 s74, s74, 4\n"
    "s_lshl_b32 s74, s74, 7\n"
    "s_add_u32 s74, s74, 0x70000\n"
    "v_add_u32 v5, s74, v3\n"
    "v_cmp_lt_u32 vcc, 15, v1\n"
    "s_nop 1\n"
    "v_cndmask_b32 v4, v4, v5, vcc\n"
    "s_lshl_b32 s75, s79, 7\n"
    "s_add_u32 s75, s75, s70\n"
    "s_add_u32 s75, s75, 0x1000\n"
    "v_add_u32 v5, s75, v3\n"
    "v_cmp_lt_u32 vcc, 23, v1\n"
    "s_nop 1\n"
    "v_cndmask_b32 v4, v4, v5, vcc\n"
    "v_not_b32 v200, v8\n"
    "s_mov_b32 exec_hi, 0\n"
    "buffer_store_dwordx4 v[200:203], v4, s[52:55], 0 offen sc1\n"
    "s_mov_b64 exec, -1\n"
    "s_waitcnt vmcnt(10)\n"
    "s_waitcnt lgkmcnt(0)\n"
    "s_barrier\n"
    "ds_read_b64_tr_b16 v[144:145], v52\n"
    "ds_read_b64_tr_b16 v[146:147], v52 offset:1024\n"
    "ds_read_b128 v[10:13], v48\n"
    "ds_read_b128 v[34:37], v48 offset:2048\n"
    "ds_read_b64_tr_b16 v[148:149], v53\n"
    "ds_read_b64_tr_b16 v[150:151], v53 offset:1024\n"
    "ds_read_b64_tr_b16 v[152:153], v54\n"
    "ds_read_b64_tr_b16 v[154:155], v54 offset:1024\n"
    "ds_read_b64_tr_b16 v[156:157], v55\n"
    "ds_read_b64_tr_b16 v[158:159], v55 offset:1024\n"
    "ds_read_b128 v[140:143], v48 offset:6144\n"
    "ds_read_b128 v[136:139], v48 offset:4096\n"
    "s_waitcnt lgkmcnt(9)\n"
    "v_mfma_f32_16x16x32_bf16 v[18:21], v[144:147], v[10:13], 0\n"
    "ds_read_b64_tr_b16 v[176:177], v56 offset:8192\n"
    "s_lshl_b32 s84, s83, 7\n"
    "s_waitcnt lgkmcnt(9)\n"
    "v_mfma_f32_16x16x32_bf16 v[26:29], v[144:147], v[34:37], 0\n"
    "ds_read_b64_tr_b16 v[178:179], v56 offset:9216\n"
    "s_add_u32 m0, s89, 0x10000\n"
    "s_waitcnt lgkmcnt(8)\n"
    "v_mfma_f32_16x16x32_bf16 v[30:33], v[148:151], v[34:37], 0\n"
    "ds_read_b128 v[160:163], v49\n"
    "s_add_u32 s84, s84, s86\n"
    "v_mfma_f32_16x16x32_bf16 v[22:25], v[148:151], v[10:13], 0\n"
    "ds_read_b128 v[164:167], v49 offset:2048\n"
    "s_waitcnt lgkmcnt(8)\n"
    "v_mfma_f32_16x16x32_bf16 v[104:107], v[152:155], v[10:13], 0\n"
    "ds_read_b64_tr_b16 v[180:181], v57 offset:8192\n"
    "buffer_load_dwordx4 v40, s[40:43], s84 offen lds\n"
    "v_mfma_f32_16x16x32_bf16 v[112:115], v[152:155], v[34:37], 0\n"
    "ds_read_b64_tr_b16 v[182:183], v57 offset:9216\n"
    "buffer_load_dwordx4 v41, s[40:43], s84 offen offset:1024 lds\n"
    "s_waitcnt lgkmcnt(8)\n"
    "v_mfma_f32_16x16x32_bf16 v[116:119], v[156:159], v[34:37], 0\n"
    "ds_read_b64_tr_b16 v[184:185], v58 offset:8192\n"
    "buffer_load_dwordx4 v42, s[40:43], s84 offen offset:2048 lds\n"
    "v_mfma_f32_16x16x32_bf16 v[108:111], v[156:159], v[10:13], 0\n"
    "ds_read_b64_tr_b16 v[186:187], v58 offset:9216\n"
    "s_nop 8\n"
    "s_waitcnt lgkmcnt(9)\n"
    "v_mfma_f32_16x16x32_bf16 v[132:135], v[156:159], v[140:143], 0\n"
    "ds_read_b64_tr_b16 v[188:189], v59 offset:8192\n"
    "s_waitcnt lgkmcnt(9)\n"
    "v_mfma_f32_16x16x32_bf16 v[124:127], v[156:159], v[136:139], 0\n"
    "ds_read_b64_tr_b16 v[190:191], v59 offset:9216\n"
    "v_mfma_f32_16x16x32_bf16 v[196:199], v[84:87], v[156:159], 0\n"
    "v_mfma_f32_16x16x32_bf16 v[120:123], v[152:155], v[136:139], 0\n"
    "ds_read_b128 v[172:175], v49 offset:6144\n"
    "buffer_load_dwordx4 v43, s[40:43], s84 offen offset:3072 lds\n"
    "v_mfma_f32_16x16x32_bf16 v[128:131], v[152:155], v[140:143], 0\n"
    "ds_read_b128 v[168:171], v49 offset:4096\n"
    "v_mfma_f32_16x16x32_bf16 v[196:199], v[80:83], v[152:155], v[196:199]\n"
    "v_mfma_f32_16x16x32_bf16 v[100:103], v[148:151], v[140:143], 0\n"
    "v_mfma_f32_16x16x32_bf16 v[92:95], v[148:151], v[136:139], 0\n"
    "v_mfma_f32_16x16x32_bf16 v[196:199], v[76:79], v[148:151], v[196:199]\n"
    "v_mfma_f32_16x16x32_bf16 v[88:91], v[144:147], v[136:139], 0\n"
    "s_mul_i32 s85, s83, 0x38000\n"
    "s_add_u32 m0, s89, 0x14000\n"
    "v_mfma_f32_16x16x32_bf16 v[96:99], v[144:147], v[140:143], 0\n"
    "s_add_u32 s85, s85, s87\n"
    "buffer_load_dwordx4 v44, s[44:47], s85 offen lds\n"
    "v_mfma_f32_16x16x32_bf16 v[196:199], v[72:75], v[144:147], v[196:199]\n"
    "s_nop 8\n"
    "s_waitcnt lgkmcnt(6)\n"
    "v_mfma_f32_16x16x32_bf16 v[18:21], v[176:179], v[160:163], v[18:21]\n"
    "v_mfma_f32_16x16x32_bf16 v[26:29], v[176:179], v[164:167], v[26:29]\n"
    "s_waitcnt vmcnt(7) lgkmcnt(0)\n"
    "s_barrier\n"
    "v_mfma_f32_16x16x32_bf16 v[30:33], v[180:183], v[164:167], v[30:33]\n"
    "ds_read_b64_tr_b16 v[144:145], v52 offset:32768\n"
    "buffer_load_dwordx4 v45, s[44:47], s85 offen offset:1024 lds\n"
    "v_mfma_f32_16x16x32_bf16 v[22:25], v[180:183], v[160:163], v[22:25]\n"
    "ds_read_b64_tr_b16 v[146:147], v52 offset:33792\n"
    "s_add_u32 s83, s83, 1\n"
    "v_mfma_f32_16x16x32_bf16 v[104:107], v[184:187], v[160:163], v[104:107]\n"
    "ds_read_b128 v[10:13], v48 offset:32768\n"
    "s_cmp_eq_u32 s83, 7\n"
    "v_mfma_f32_16x16x32_bf16 v[112:115], v[184:187], v[164:167], v[112:115]\n"
    "ds_read_b128 v[34:37], v48 offset:34816\n"
    "s_cselect_b32 s83, 0, s83\n"
    "v_mfma_f32_16x16x32_bf16 v[116:119], v[188:191], v[164:167], v[116:119]\n"
    "ds_read_b64_tr_b16 v[148:149], v53 offset:32768\n"
    "buffer_load_dwordx4 v46, s[44:47], s85 offen offset:2048 lds\n"
    "v_mfma_f32_16x16x32_bf16 v[108:111], v[188:191], v[160:163], v[108:111]\n"
    "ds_read_b64_tr_b16 v[150:151], v53 offset:33792\n"
    "s_nop 8\n"
    "v_mfma_f32_16x16x32_bf16 v[132:135], v[188:191], v[172:175], v[132:135]\n"
    "ds_read_b64_tr_b16 v[152:153], v54 offset:32768\n"
    "v_mfma_f32_16x16x32_bf16 v[124:127], v[188:191], v[168:171], v[124:127]\n"
    "ds_read_b64_tr_b16 v[154:155], v54 offset:33792\n"
    "v_mfma_f32_16x16x32_bf16 v[120:123], v[184:187], v[168:171], v[120:123]\n"
    "ds_read_b64_tr_b16 v[156:157], v55 offset:32768\n"
    "buffer_load_dwordx4 v47, s[44:47], s85 offen offset:3072 lds\n"
    "v_mfma_f32_16x16x32_bf16 v[128:131], v[184:187], v[172:175], v[128:131]\n"
    "ds_read_b64_tr_b16 v[158:159], v55 offset:33792\n"
    "v_mfma_f32_16x16x32_bf16 v[100:103], v[180:183], v[172:175], v[100:103]\n"
    "ds_read_b128 v[140:143], v48 offset:38912\n"
    "v_mfma_f32_16x16x32_bf16 v[92:95], v[180:183], v[168:171], v[92:95]\n"
    "ds_read_b128 v[136:139], v48 offset:36864\n"
    "v_mfma_f32_16x16x32_bf16 v[88:91], v[176:179], v[168:171], v[88:91]\n"
    "s_lshl_b32 s84, s83, 7\n"
    "s_add_u32 m0, s89, 0\n"
    "v_mfma_f32_16x16x32_bf16 v[96:99], v[176:179], v[172:175], v[96:99]\n"
    "s_add_u32 s84, s84, s86\n"
    "s_nop 8\n"
    "s_waitcnt lgkmcnt(6)\n"
    "v_mfma_f32_16x16x32_bf16 v[18:21], v[144:147], v[10:13], v[18:21]\n"
    "ds_read_b64_tr_b16 v[176:177], v56 offset:40960\n"
    "buffer_load_dwordx4 v40, s[40:43], s84 offen lds\n"
    "v_mfma_f32_16x16x32_bf16 v[26:29], v[144:147], v[34:37], v[26:29]\n"
    "ds_read_b64_tr_b16 v[178:179], v56 offset:41984\n"
    "v_mfma_f32_16x16x32_bf16 v[30:33], v[148:151], v[34:37], v[30:33]\n"
    "ds_read_b128 v[160:163], v49 offset:32768\n"
    "buffer_load_dwordx4 v41, s[40:43], s84 offen offset:1024 lds\n"
    "v_mfma_f32_16x16x32_bf16 v[22:25], v[148:151], v[10:13], v[22:25]\n"
    "ds_read_b128 v[164:167], v49 offset:34816\n"
    "s_waitcnt lgkmcnt(4)\n"
    "v_mfma_f32_16x16x32_bf16 v[104:107], v[152:155], v[10:13], v[104:107]\n"
    "ds_read_b64_tr_b16 v[180:181], v57 offset:40960\n"
    "v_mfma_f32_16x16x32_bf16 v[112:115], v[152:155], v[34:37], v[112:115]\n"
    "ds_read_b64_tr_b16 v[182:183], v57 offset:41984\n"
    "v_mfma_f32_16x16x32_bf16 v[116:119], v[156:159], v[34:37], v[116:119]\n"
    "ds_read_b64_tr_b16 v[184:185], v58 offset:40960\n"
    "buffer_load_dwordx4 v42, s[40:43], s84 offen offset:2048 lds\n"
    "v_mfma_f32_16x16x32_bf16 v[108:111], v[156:159], v[10:13], v[108:111]\n"
    "ds_read_b64_tr_b16 v[186:187], v58 offset:41984\n"
    "s_nop 8\n"
    "v_mfma_f32_16x16x32_bf16 v[132:135], v[156:159], v[140:143], v[132:135]\n"
    "ds_read_b64_tr_b16 v[188:189], v59 offset:40960\n"
    "buffer_load_dwordx4 v43, s[40:43], s84 offen offset:3072 lds\n"
    "v_mfma_f32_16x16x32_bf16 v[124:127], v[156:159], v[136:139], v[124:127]\n"
    "ds_read_b64_tr_b16 v[190:191], v59 offset:41984\n"
    "v_mfma_f32_16x16x32_bf16 v[196:199], v[84:87], v[156:159], v[196:199]\n"
    "v_mfma_f32_16x16x32_bf16 v[120:123], v[152:155], v[136:139], v[120:123]\n"
    "ds_read_b128 v[172:175], v49 offset:38912\n"
    "v_mfma_f32_16x16x32_bf16 v[128:131], v[152:155], v[140:143], v[128:131]\n"
    "ds_read_b128 v[168:171], v49 offset:36864\n"
    "v_mfma_f32_16x16x32_bf16 v[196:199], v[80:83], v[152:155], v[196:199]\n"
    "v_mfma_f32_16x16x32_bf16 v[100:103], v[148:151], v[140:143], v[100:103]\n"
    "s_mul_i32 s85, s83, 0x38000\n"
    "s_add_u32 m0, s89, 0x4000\n"
    "v_mfma_f32_16x16x32_bf16 v[92:95], v[148:151], v[136:139], v[92:95]\n"
    "s_add_u32 s85, s85, s87\n"
    "buffer_load_dwordx4 v44, s[44:47], s85 offen lds\n"
    "v_mfma_f32_16x16x32_bf16 v[196:199], v[76:79], v[148:151], v[196:199]\n"
    "v_mfma_f32_16x16x32_bf16 v[88:91], v[144:147], v[136:139], v[88:91]\n"
    "v_mfma_f32_16x16x32_bf16 v[96:99], v[144:147], v[140:143], v[96:99]\n"
    "v_mfma_f32_16x16x32_bf16 v[196:199], v[72:75], v[144:147], v[196:199]\n"
    "s_nop 8\n"
    "s_waitcnt lgkmcnt(6)\n"
    "v_mfma_f32_16x16x32_bf16 v[18:21], v[176:179], v[160:163], v[18:21]\n"
    "buffer_load_dwordx4 v45, s[44:47], s85 offen offset:1024 lds\n"
    "s_add_u32 s83, s83, 1\n"
    "v_mfma_f32_16x16x32_bf16 v[26:29], v[176:179], v[164:167], v[26:29]\n"
    "s_waitcnt vmcnt(6) lgkmcnt(0)\n"
    "s_barrier\n"
    "v_mfma_f32_16x16x32_bf16 v[30:33], v[180:183], v[164:167], v[30:33]\n"
    "ds_read_b64_tr_b16 v[144:145], v60\n"
    "s_cmp_eq_u32 s83, 7\n"
    "v_mfma_f32_16x16x32_bf16 v[22:25], v[180:183], v[160:163], v[22:25]\n"
    "ds_read_b64_tr_b16 v[146:147], v60 offset:1024\n"
    "s_cselect_b32 s83, 0, s83\n"
    "v_mfma_f32_16x16x32_bf16 v[104:107], v[184:187], v[160:163], v[104:107]\n"
    "ds_read_b128 v[10:13], v50\n"
    "buffer_load_dwordx4 v46, s[44:47], s85 offen offset:2048 lds\n"
    "v_mfma_f32_16x16x32_bf16 v[112:115], v[184:187], v[164:167], v[112:115]\n"
    "ds_read_b128 v[34:37], v50 offset:2048\n"
    "v_mfma_f32_16x16x32_bf16 v[116:119], v[188:191], v[164:167], v[116:119]\n"
    "ds_read_b64_tr_b16 v[148:149], v61\n"
    "v_mfma_f32_16x16x32_bf16 v[108:111], v[188:191], v[160:163], v[108:111]\n"
    "ds_read_b64_tr_b16 v[150:151], v61 offset:1024\n"
    "s_nop 8\n"
    "v_mfma_f32_16x16x32_bf16 v[132:135], v[188:191], v[172:175], v[132:135]\n"
    "ds_read_b64_tr_b16 v[152:153], v62\n"
    "buffer_load_dwordx4 v47, s[44:47], s85 offen offset:3072 lds\n"
    "v_mfma_f32_16x16x32_bf16 v[124:127], v[188:191], v[168:171], v[124:127]\n"
    "ds_read_b64_tr_b16 v[154:155], v62 offset:1024\n"
    "v_mfma_f32_16x16x32_bf16 v[120:123], v[184:187], v[168:171], v[120:123]\n"
    "ds_read_b64_tr_b16 v[156:157], v63\n"
    "v_mfma_f32_16x16x32_bf16 v[128:131], v[184:187], v[172:175], v[128:131]\n"
    "ds_read_b64_tr_b16 v[158:159], v63 offset:1024\n"
    "v_mfma_f32_16x16x32_bf16 v[100:103], v[180:183], v[172:175], v[100:103]\n"
    "ds_read_b128 v[140:143], v50 offset:6144\n"
    "s_lshl_b32 s84, s83, 7\n"
    "v_mfma_f32_16x16x32_bf16 v[92:95], v[180:183], v[168:171], v[92:95]\n"
    "ds_read_b128 v[136:139], v50 offset:4096\n"
    "s_add_u32 m0, s89, 0x8000\n"
    "v_mfma_f32_16x16x32_bf16 v[88:91], v[176:179], v[168:171], v[88:91]\n"
    "s_add_u32 s84, s84, s86\n"
    "buffer_load_dwordx4 v40, s[40:43], s84 offen lds\n"
    "v_mfma_f32_16x16x32_bf16 v[96:99], v[176:179], v[172:175], v[96:99]\n"
    "s_nop 8\n"
    "s_waitcnt lgkmcnt(6)\n"
    "v_mfma_f32_16x16x32_bf16 v[18:21], v[144:147], v[10:13], v[18:21]\n"
    "ds_read_b64_tr_b16 v[176:177], v64 offset:8192\n"
    "buffer_load_dwordx4 v41, s[40:43], s84 offen offset:1024 lds\n"
    "v_mfma_f32_16x16x32_bf16 v[26:29], v[144:147], v[34:37], v[26:29]\n"
    "ds_read_b64_tr_b16 v[178:179], v64 offset:9216\n"
    "v_mfma_f32_16x16x32_bf16 v[30:33], v[148:151], v[34:37], v[30:33]\n"
    "ds_read_b128 v[160:163], v51\n"
    "v_mfma_f32_16x16x32_bf16 v[22:25], v[148:151], v[10:13], v[22:25]\n"
    "ds_read_b128 v[164:167], v51 offset:2048\n"
    "s_waitcnt lgkmcnt(4)\n"
    "v_mfma_f32_16x16x32_bf16 v[104:107], v[152:155], v[10:13], v[104:107]\n"
    "ds_read_b64_tr_b16 v[180:181], v65 offset:8192\n"
    "buffer_load_dwordx4 v42, s[40:43], s84 offen offset:2048 lds\n"
    "v_mfma_f32_16x16x32_bf16 v[112:115], v[152:155], v[34:37], v[112:115]\n"
    "ds_read_b64_tr_b16 v[182:183], v65 offset:9216\n"
    "v_mfma_f32_16x16x32_bf16 v[116:119], v[156:159], v[34:37], v[116:119]\n"
    "ds_read_b64_tr_b16 v[184:185], v66 offset:8192\n"
    "v_mfma_f32_16x16x32_bf16 v[108:111], v[156:159], v[10:13], v[108:111]\n"
    "ds_read_b64_tr_b16 v[186:187], v66 offset:9216\n"
    "s_nop 8\n"
    "v_mfma_f32_16x16x32_bf16 v[132:135], v[156:159], v[140:143], v[132:135]\n"
    "ds_read_b64_tr_b16 v[188:189], v67 offset:8192\n"
    "buffer_load_dwordx4 v43, s[40:43], s84 offen offset:3072 lds\n"
    "v_mfma_f32_16x16x32_bf16 v[124:127], v[156:159], v[136:139], v[124:127]\n"
    "ds_read_b64_tr_b16 v[190:191], v67 offset:9216\n"
    "v_mfma_f32_16x16x32_bf16 v[196:199], v[84:87], v[156:159], v[196:199]\n"
    "v_mfma_f32_16x16x32_bf16 v[120:123], v[152:155], v[136:139], v[120:123]\n"
    "ds_read_b128 v[172:175], v51 offset:6144\n"
    "s_mul_i32 s85, s83, 0x38000\n"
    "v_mfma_f32_16x16x32_bf16 v[128:131], v[152:155], v[140:143], v[128:131]\n"
    "ds_read_b128 v[168:171], v51 offset:4096\n"
    "s_add_u32 m0, s89, 0xc000\n"
    "v_mfma_f32_16x16x32_bf16 v[196:199], v[80:83], v[152:155], v[196:199]\n"
    "s_add_u32 s85, s85, s87\n"
    "buffer_load_dwordx4 v44, s[44:47], s85 offen lds\n"
    "v_mfma_f32_16x16x32_bf16 v[100:103], v[148:151], v[140:143], v[100:103]\n"
    "v_mfma_f32_16x16x32_bf16 v[92:95], v[148:151], v[136:139], v[92:95]\n"
    "v_mfma_f32_16x16x32_bf16 v[196:199], v[76:79], v[148:151], v[196:199]\n"
    "v_mfma_f32_16x16x32_bf16 v[88:91], v[144:147], v[136:139], v[88:91]\n"
    "buffer_load_dwordx4 v45, s[44:47], s85 offen offset:1024 lds\n"
    "s_add_u32 s83, s83, 1\n"
    "v_mfma_f32_16x16x32_bf16 v[96:99], v[144:147], v[140:143], v[96:99]\n"
    "s_cmp_eq_u32 s83, 7\n"
    "s_cselect_b32 s83, 0, s83\n"
    "v_mfma_f32_16x16x32_bf16 v[196:199], v[72:75], v[144:147], v[196:199]\n"
    "s_nop 8\n"
    "s_waitcnt lgkmcnt(6)\n"
    "v_mfma_f32_16x16x32_bf16 v[18:21], v[176:179], v[160:163], v[18:21]\n"
    "v_mfma_f32_16x16x32_bf16 v[26:29], v[176:179], v[164:167], v[26:29]\n"
    "s_waitcnt vmcnt(6) lgkmcnt(0)\n"
    "s_barrier\n"
    "v_mfma_f32_16x16x32_bf16 v[30:33], v[180:183], v[164:167], v[30:33]\n"
    "ds_read_b64_tr_b16 v[144:145], v52\n"
    "buffer_load_dwordx4 v46, s[44:47], s85 offen offset:2048 lds\n"
    "v_mfma_f32_16x16x32_bf16 v[22:25], v[180:183], v[160:163], v[22:25]\n"
    "ds_read_b64_tr_b16 v[146:147], v52 offset:1024\n"
    "v_mfma_f32_16x16x32_bf16 v[104:107], v[184:187], v[160:163], v[104:107]\n"
    "ds_read_b128 v[10:13], v48\n"
    "v_mfma_f32_16x16x32_bf16 v[112:115], v[184:187], v[164:167], v[112:115]\n"
    "ds_read_b128 v[34:37], v48 offset:2048\n"
    "v_mfma_f32_16x16x32_bf16 v[116:119], v[188:191], v[164:167], v[116:119]\n"
    "ds_read_b64_tr_b16 v[148:149], v53\n"
    "buffer_load_dwordx4 v47, s[44:47], s85 offen offset:3072 lds\n"
    "v_mfma_f32_16x16x32_bf16 v[108:111], v[188:191], v[160:163], v[108:111]\n"
    "ds_read_b64_tr_b16 v[150:151], v53 offset:1024\n"
    "s_nop 8\n"
    "v_mfma_f32_16x16x32_bf16 v[132:135], v[188:191], v[172:175], v[132:135]\n"
    "ds_read_b64_tr_b16 v[152:153], v54\n"
    "buffer_load_dwordx2 v[200:201], v38, s[52:55], s72 offen sc1\n"
    "v_mfma_f32_16x16x32_bf16 v[124:127], v[188:191], v[168:171], v[124:127]\n"
    "ds_read_b64_tr_b16 v[154:155], v54 offset:1024\n"
    "buffer_load_dwordx2 v[202:203], v38, s[52:55], s72 offen offset:128 sc1\n"
    "v_mfma_f32_16x16x32_bf16 v[120:123], v[184:187], v[168:171], v[120:123]\n"
    "ds_read_b64_tr_b16 v[156:157], v55\n"
    "buffer_load_dwordx2 v[204:205], v38, s[52:55], s73 offen sc1\n"
    "v_mfma_f32_16x16x32_bf16 v[128:131], v[184:187], v[172:175], v[128:131]\n"
    "ds_read_b64_tr_b16 v[158:159], v55 offset:1024\n"
    "buffer_load_dwordx2 v[206:207], v38, s[52:55], s73 offen offset:128 sc1\n"
    "v_mfma_f32_16x16x32_bf16 v[100:103], v[180:183], v[172:175], v[100:103]\n"
    "ds_read_b128 v[140:143], v48 offset:6144\n"
    "s_lshl_b32 s84, s83, 7\n"
    "v_mfma_f32_16x16x32_bf16 v[92:95], v[180:183], v[168:171], v[92:95]\n"
    "ds_read_b128 v[136:139], v48 offset:4096\n"
    "s_add_u32 m0, s89, 0x10000\n"
    "v_mfma_f32_16x16x32_bf16 v[88:91], v[176:179], v[168:171], v[88:91]\n"
    "s_add_u32 s84, s84, s86\n"
    "buffer_load_dwordx4 v40, s[40:43], s84 offen lds\n"
    "v_mfma_f32_16x16x32_bf16 v[96:99], v[176:179], v[172:175], v[96:99]\n"
    "buffer_load_dwordx4 v41, s[40:43], s84 offen offset:1024 lds\n"
    "s_nop 8\n"
    "s_waitcnt lgkmcnt(6)\n"
    "v_mfma_f32_16x16x32_bf16 v[18:21], v[144:147], v[10:13], v[18:21]\n"
    "ds_read_b64_tr_b16 v[176:177], v56 offset:8192\n"
    "v_mfma_f32_16x16x32_bf16 v[26:29], v[144:147], v[34:37], v[26:29]\n"
    "ds_read_b64_tr_b16 v[178:179], v56 offset:9216\n"
    "v_mfma_f32_16x16x32_bf16 v[30:33], v[148:151], v[34:37], v[30:33]\n"
    "ds_read_b128 v[160:163], v49\n"
    "buffer_load_dwordx4 v42, s[40:43], s84 offen offset:2048 lds\n"
    "v_mfma_f32_16x16x32_bf16 v[22:25], v[148:151], v[10:13], v[22:25]\n"
    "ds_read_b128 v[164:167], v49 offset:2048\n"
    "s_waitcnt lgkmcnt(4)\n"
    "v_mfma_f32_16x16x32_bf16 v[104:107], v[152:155], v[10:13], v[104:107]\n"
    "ds_read_b64_tr_b16 v[180:181], v57 offset:8192\n"
    "v_mfma_f32_16x16x32_bf16 v[112:115], v[152:155], v[34:37], v[112:115]\n"
    "ds_read_b64_tr_b16 v[182:183], v57 offset:9216\n"
    "v_mfma_f32_16x16x32_bf16 v[116:119], v[156:159], v[34:37], v[116:119]\n"
    "ds_read_b64_tr_b16 v[184:185], v58 offset:8192\n"
    "buffer_load_dwordx4 v43, s[40:43], s84 offen offset:3072 lds\n"
    "v_mfma_f32_16x16x32_bf16 v[108:111], v[156:159], v[10:13], v[108:111]\n"
    "ds_read_b64_tr_b16 v[186:187], v58 offset:9216\n"
    "s_nop 2\n"
    "v_mfma_f32_16x16x32_bf16 v[132:135], v[156:159], v[140:143], v[132:135]\n"
    "ds_read_b64_tr_b16 v[188:189], v59 offset:8192\n"
    "v_mfma_f32_16x16x32_bf16 v[124:127], v[156:159], v[136:139], v[124:127]\n"
    "ds_read_b64_tr_b16 v[190:191], v59 offset:9216\n"
    "v_mfma_f32_16x16x32_bf16 v[196:199], v[84:87], v[156:159], v[196:199]\n"
    "v_mfma_f32_16x16x32_bf16 v[120:123], v[152:155], v[136:139], v[120:123]\n"
    "ds_read_b128 v[172:175], v49 offset:6144\n"
    "s_mul_i32 s85, s83, 0x38000\n"
    "v_mfma_f32_16x16x32_bf16 v[128:131], v[152:155], v[140:143], v[128:131]\n"
    "ds_read_b128 v[168:171], v49 offset:4096\n"
    "s_add_u32 m0, s89, 0x14000\n"
    "v_mfma_f32_16x16x32_bf16 v[196:199], v[80:83], v[152:155], v[196:199]\n"
    "s_add_u32 s85, s85, s87\n"
    "buffer_load_dwordx4 v44, s[44:47], s85 offen lds\n"
    "v_mfma_f32_16x16x32_bf16 v[100:103], v[148:151], v[140:143], v[100:103]\n"
    "v_mfma_f32_16x16x32_bf16 v[92:95], v[148:151], v[136:139], v[92:95]\n"
    "v_mfma_f32_16x16x32_bf16 v[196:199], v[76:79], v[148:151], v[196:199]\n"
    "v_mfma_f32_16x16x32_bf16 v[88:91], v[144:147], v[136:139], v[88:91]\n"
    "buffer_load_dwordx4 v45, s[44:47], s85 offen offset:1024 lds\n"
    "s_add_u32 s83, s83, 1\n"
    "v_mfma_f32_16x16x32_bf16 v[96:99], v[144:147], v[140:143], v[96:99]\n"
    "s_cmp_eq_u32 s83, 7\n"
    "s_cselect_b32 s83, 0, s83\n"
    "v_mfma_f32_16x16x32_bf16 v[196:199], v[72:75], v[144:147], v[196:199]\n"
    "s_nop 2\n"
    "s_waitcnt lgkmcnt(6)\n"
    "v_mfma_f32_16x16x32_bf16 v[18:21], v[176:179], v[160:163], v[18:21]\n"
    "buffer_load_dwordx4 v46, s[44:47], s85 offen offset:2048 lds\n"
    "v_mfma_f32_16x16x32_bf16 v[26:29], v[176:179], v[164:167], v[26:29]\n"
    "s_waitcnt vmcnt(11) lgkmcnt(0)\n"
    "s_barrier\n"
    "v_mfma_f32_16x16x32_bf16 v[30:33], v[180:183], v[164:167], v[30:33]\n"
    "ds_read_b64_tr_b16 v[144:145], v52 offset:32768\n"
    "v_mfma_f32_16x16x32_bf16 v[22:25], v[180:183], v[160:163], v[22:25]\n"
    "ds_read_b64_tr_b16 v[146:147], v52 offset:33792\n"
    "v_mfma_f32_16x16x32_bf16 v[104:107], v[184:187], v[160:163], v[104:107]\n"
    "ds_read_b128 v[10:13], v48 offset:32768\n"
    "buffer_load_dwordx4 v47, s[44:47], s85 offen offset:3072 lds\n"
    "v_mfma_f32_16x16x32_bf16 v[112:115], v[184:187], v[164:167], v[112:115]\n"
    "ds_read_b128 v[34:37], v48 offset:34816\n"
    "v_mfma_f32_16x16x32_bf16 v[116:119], v[188:191], v[164:167], v[116:119]\n"
    "ds_read_b64_tr_b16 v[148:149], v53 offset:32768\n"
    "v_mfma_f32_16x16x32_bf16 v[108:111], v[188:191], v[160:163], v[108:111]\n"
    "ds_read_b64_tr_b16 v[150:151], v53 offset:33792\n"
    "s_nop 2\n"
    "v_mfma_f32_16x16x32_bf16 v[132:135], v[188:191], v[172:175], v[132:135]\n"
    "ds_read_b64_tr_b16 v[152:153], v54 offset:32768\n"
    "s_lshl_b32 s84, s83, 7\n"
    "v_mfma_f32_16x16x32_bf16 v[124:127], v[188:191], v[168:171], v[124:127]\n"
    "ds_read_b64_tr_b16 v[154:155], v54 offset:33792\n"
    "s_add_u32 m0, s89, 0\n"
    "v_mfma_f32_16x16x32_bf16 v[120:123], v[184:187], v[168:171], v[120:123]\n"
    "ds_read_b64_tr_b16 v[156:157], v55 offset:32768\n"
    "s_add_u32 s84, s84, s86\n"
    "v_mfma_f32_16x16x32_bf16 v[128:131], v[184:187], v[172:175], v[128:131]\n"
    "ds_read_b64_tr_b16 v[158:159], v55 offset:33792\n"
    "buffer_load_dwordx4 v40, s[40:43], s84 offen lds\n"
    "v_mfma_f32_16x16x32_bf16 v[100:103], v[180:183], v[172:175], v[100:103]\n"
    "ds_read_b128 v[140:143], v48 offset:38912\n"
    "buffer_load_dwordx4 v41, s[40:43], s84 offen offset:1024 lds\n"
    "v_mfma_f32_16x16x32_bf16 v[92:95], v[180:183], v[168:171], v[92:95]\n"
    "ds_read_b128 v[136:139], v48 offset:36864\n"
    "v_mfma_f32_16x16x32_bf16 v[88:91], v[176:179], v[168:171], v[88:91]\n"
    "v_mfma_f32_16x16x32_bf16 v[96:99], v[176:179], v[172:175], v[96:99]\n"
    "s_nop 2\n"
    "s_waitcnt lgkmcnt(6)\n"
    "v_mfma_f32_16x16x32_bf16 v[18:21], v[144:147], v[10:13], v[18:21]\n"
    "ds_read_b64_tr_b16 v[176:177], v56 offset:40960\n"
    "buffer_load_dwordx4 v42, s[40:43], s84 offen offset:2048 lds\n"
    "v_mfma_f32_16x16x32_bf16 v[26:29], v[144:147], v[34:37], v[26:29]\n"
    "ds_read_b64_tr_b16 v[178:179], v56 offset:41984\n"
    "v_mfma_f32_16x16x32_bf16 v[30:33], v[148:151], v[34:37], v[30:33]\n"
    "ds_read_b128 v[160:163], v49 offset:32768\n"
    "v_mfma_f32_16x16x32_bf16 v[22:25], v[148:151], v[10:13], v[22:25]\n"
    "ds_read_b128 v[164:167], v49 offset:34816\n"
    "s_waitcnt lgkmcnt(4)\n"
    "v_mfma_f32_16x16x32_bf16 v[104:107], v[152:155], v[10:13], v[104:107]\n"
    "ds_read_b64_tr_b16 v[180:181], v57 offset:40960\n"
    "buffer_load_dwordx4 v43, s[40:43], s84 offen offset:3072 lds\n"
    "v_mfma_f32_16x16x32_bf16 v[112:115], v[152:155], v[34:37], v[112:115]\n"
    "ds_read_b64_tr_b16 v[182:183], v57 offset:41984\n"
    "v_mfma_f32_16x16x32_bf16 v[116:119], v[156:159], v[34:37], v[116:119]\n"
    "ds_read_b64_tr_b16 v[184:185], v58 offset:40960\n"
    "v_mfma_f32_16x16x32_bf16 v[108:111], v[156:159], v[10:13], v[108:111]\n"
    "ds_read_b64_tr_b16 v[186:187], v58 offset:41984\n"
    "v_mfma_f32_16x16x32_bf16 v[132:135], v[156:159], v[140:143], v[132:135]\n"
    "ds_read_b64_tr_b16 v[188:189], v59 offset:40960\n"
    "s_mul_i32 s85, s83, 0x38000\n"
    "v_mfma_f32_16x16x32_bf16 v[124:127], v[156:159], v[136:139], v[124:127]\n"
    "ds_read_b64_tr_b16 v[190:191], v59 offset:41984\n"
    "s_add_u32 m0, s89, 0x4000\n"
    "v_mfma_f32_16x16x32_bf16 v[196:199], v[84:87], v[156:159], v[196:199]\n"
    "s_add_u32 s85, s85, s87\n"
    "buffer_load_dwordx4 v44, s[44:47], s85 offen lds\n"
    "v_mfma_f32_16x16x32_bf16 v[120:123], v[152:155], v[136:139], v[120:123]\n"
    "ds_read_b128 v[172:175], v49 offset:38912\n"
    "s_waitcnt vmcnt(16)\n"
    "v_xor_b32 v3, s76, v201\n"
    "v_mfma_f32_16x16x32_bf16 v[128:131], v[152:155], v[140:143], v[128:131]\n"
    "ds_read_b128 v[168:171], v49 offset:36864\n"
    "s_waitcnt vmcnt(15)\n"
    "v_xor_b32 v4, s76, v203\n"
    "v_mfma_f32_16x16x32_bf16 v[196:199], v[80:83], v[152:155], v[196:199]\n"
    "s_waitcnt vmcnt(14)\n"
    "v_xor_b32 v5, s76, v205\n"
    "v_or3_b32 v3, v3, v4, v5\n"
    "v_mfma_f32_16x16x32_bf16 v[100:103], v[148:151], v[140:143], v[100:103]\n"
    "s_waitcnt vmcnt(13)\n"
    "v_xor_b32 v4, s76, v207\n"
    "v_or_b32 v3, v3, v4\n"
    "v_mfma_f32_16x16x32_bf16 v[92:95], v[148:151], v[136:139], v[92:95]\n"
    "v_cmp_ne_u32 vcc, 0, v3\n"
    "s_nop 0\n"
    "s_cbranch_vccnz .Lslowe%=\n"
    ".Loke%=:\n"
    "s_mov_b32 s74, 0x1000100\n"
    "v_mfma_f32_16x16x32_bf16 v[196:199], v[76:79], v[148:151], v[196:199]\n"
    "v_mov_b32 v201, v200\n"
    "v_perm_b32 v200, v200, v200, s74\n"
    "v_mfma_f32_16x16x32_bf16 v[88:91], v[144:147], v[136:139], v[88:91]\n"
    "buffer_load_dwordx4 v45, s[44:47], s85 offen offset:1024 lds\n"
    "s_add_u32 s83, s83, 1\n"
    "v_mfma_f32_16x16x32_bf16 v[96:99], v[144:147], v[140:143], v[96:99]\n"
    "s_cmp_eq_u32 s83, 7\n"
    "s_cselect_b32 s83, 0, s83\n"
    "v_mfma_f32_16x16x32_bf16 v[196:199], v[72:75], v[144:147], v[196:199]\n"
    "v_mov_b32 v203, v202\n"
    "s_waitcnt lgkmcnt(6)\n"
    "v_mfma_f32_16x16x32_bf16 v[18:21], v[176:179], v[160:163], v[18:21]\n"
    "v_perm_b32 v202, v202, v202, s74\n"
    "v_mov_b32 v205, v204\n"
    "v_mfma_f32_16x16x32_bf16 v[26:29], v[176:179], v[164:167], v[26:29]\n"
    "s_waitcnt vmcnt(6) lgkmcnt(0)\n"
    "s_barrier\n"
    "v_mfma_f32_16x16x32_bf16 v[30:33], v[180:183], v[164:167], v[30:33]\n"
    "ds_read_b64_tr_b16 v[144:145], v60\n"
    "v_perm_b32 v204, v204, v204, s74\n"
    "v_mfma_f32_16x16x32_bf16 v[22:25], v[180:183], v[160:163], v[22:25]\n"
    "ds_read_b64_tr_b16 v[146:147], v60 offset:1024\n"
    "buffer_load_dwordx4 v46, s[44:47], s85 offen offset:2048 lds\n"
    "v_mfma_f32_16x16x32_bf16 v[104:107], v[184:187], v[160:163], v[104:107]\n"
    "ds_read_b128 v[10:13], v50\n"
    "v_mov_b32 v207, v206\n"
    "v_mfma_f32_16x16x32_bf16 v[112:115], v[184:187], v[164:167], v[112:115]\n"
    "ds_read_b128 v[34:37], v50 offset:2048\n"
    "v_perm_b32 v206, v206, v206, s74\n"
    "v_mfma_f32_16x16x32_bf16 v[116:119], v[188:191], v[164:167], v[116:119]\n"
    "ds_read_b64_tr_b16 v[148:149], v61\n"
    "buffer_load_dwordx4 v47, s[44:47], s85 offen offset:3072 lds\n"
    "v_mfma_f32_16x16x32_bf16 v[108:111], v[188:191], v[160:163], v[108:111]\n"
    "ds_read_b64_tr_b16 v[150:151], v61 offset:1024\n"
    "v_mfma_f32_16x16x32_bf16 v[132:135], v[188:191], v[172:175], v[132:135]\n"
    "ds_read_b64_tr_b16 v[152:153], v62\n"
    "s_mul_i32 s84, s93, 0x1c000\n"
    "v_mfma_f32_16x16x32_bf16 v[124:127], v[188:191], v[168:171], v[124:127]\n"
    "ds_read_b64_tr_b16 v[154:155], v62 offset:1024\n"
    "s_add_u32 m0, s89, 0x8000\n"
    "v_mfma_f32_16x16x32_bf16 v[120:123], v[184:187], v[168:171], v[120:123]\n"
    "ds_read_b64_tr_b16 v[156:157], v63\n"
    "s_add_u32 s84, s84, s92\n"
    "v_mfma_f32_16x16x32_bf16 v[128:131], v[184:187], v[172:175], v[128:131]\n"
    "ds_read_b64_tr_b16 v[158:159], v63 offset:1024\n"
    "buffer_load_dwordx4 v192, s[48:51], s84 offen lds\n"
    "v_mfma_f32_16x16x32_bf16 v[100:103], v[180:183], v[172:175], v[100:103]\n"
    "ds_read_b128 v[140:143], v50 offset:6144\n"
    "s_mov_b32 s96, 0xffff\n"
    "v_mfma_f32_16x16x32_bf16 v[92:95], v[180:183], v[168:171], v[92:95]\n"
    "ds_read_b128 v[136:139], v50 offset:4096\n"
    "s_mov_b32 s97, 0\n"
    "v_mfma_f32_16x16x32_bf16 v[88:91], v[176:179], v[168:171], v[88:91]\n"
    "s_mov_b32 s98, 0xffff0000\n"
    "s_mov_b32 s99, 0\n"
    "v_mfma_f32_16x16x32_bf16 v[96:99], v[176:179], v[172:175], v[96:99]\n"
    "buffer_load_dwordx4 v193, s[48:51], s84 offen offset:1024 lds\n"
    "s_waitcnt lgkmcnt(6)\n"
    "v_mfma_f32_16x16x32_bf16 v[18:21], v[144:147], v[10:13], v[18:21]\n"
    "ds_read_b64_tr_b16 v[176:177], v64 offset:8192\n"
    "s_add_u32 s93, s93, 1\n"
    "v_mfma_f32_16x16x32_bf16 v[26:29], v[144:147], v[34:37], v[26:29]\n"
    "ds_read_b64_tr_b16 v[178:179], v64 offset:9216\n"
    "s_cmp_eq_u32 s93, 7\n"
    "v_mfma_f32_16x16x32_bf16 v[30:33], v[148:151], v[34:37], v[30:33]\n"
    "ds_read_b128 v[160:163], v51\n"
    "s_cselect_b32 s93, 0, s93\n"
    "v_mfma_f32_16x16x32_bf16 v[22:25], v[148:151], v[10:13], v[22:25]\n"
    "ds_read_b128 v[164:167], v51 offset:2048\n"
    "s_waitcnt lgkmcnt(4)\n"
    "v_mfma_f32_16x16x32_bf16 v[104:107], v[152:155], v[10:13], v[104:107]\n"
    "ds_read_b64_tr_b16 v[180:181], v65 offset:8192\n"
    "s_mov_b32 s90, 0\n"
    "v_mfma_f32_16x16x32_bf16 v[112:115], v[152:155], v[34:37], v[112:115]\n"
    "ds_read_b64_tr_b16 v[182:183], v65 offset:9216\n"
    "s_mov_b32 s91, 0xffff\n"
    "v_mfma_f32_16x16x32_bf16 v[116:119], v[156:159], v[34:37], v[116:119]\n"
    "ds_read_b64_tr_b16 v[184:185], v66 offset:8192\n"
    "s_mov_b32 s56, 0\n"
    "v_mfma_f32_16x16x32_bf16 v[108:111], v[156:159], v[10:13], v[108:111]\n"
    "ds_read_b64_tr_b16 v[186:187], v66 offset:9216\n"
    "s_mov_b32 s57, 0xffff0000\n"
    "v_mfma_f32_16x16x32_bf16 v[132:135], v[156:159], v[140:143], v[132:135]\n"
    "ds_read_b64_tr_b16 v[188:189], v67 offset:8192\n"
    "buffer_load_dwordx4 v194, s[48:51], s84 offen offset:2048 lds\n"
    "v_mfma_f32_16x16x32_bf16 v[124:127], v[156:159], v[136:139], v[124:127]\n"
    "ds_read_b64_tr_b16 v[190:191], v67 offset:9216\n"
    "buffer_load_dwordx4 v195, s[48:51], s84 offen offset:3072 lds\n"
    "v_mfma_f32_16x16x32_bf16 v[196:199], v[84:87], v[156:159], v[196:199]\n"
    "v_mfma_f32_16x16x32_bf16 v[120:123], v[152:155], v[136:139], v[120:123]\n"
    "ds_read_b128 v[172:175], v51 offset:6144\n"
    "s_mul_i32 s84, s93, 0x1c000\n"
    "v_mfma_f32_16x16x32_bf16 v[128:131], v[152:155], v[140:143], v[128:131]\n"
    "ds_read_b128 v[168:171], v51 offset:4096\n"
    "s_add_u32 m0, s89, 0xc000\n"
    "v_mfma_f32_16x16x32_bf16 v[196:199], v[80:83], v[152:155], v[196:199]\n"
    "s_add_u32 s84, s84, s92\n"
    "buffer_load_dwordx4 v192, s[48:51], s84 offen lds\n"
    "v_mfma_f32_16x16x32_bf16 v[100:103], v[148:151], v[140:143], v[100:103]\n"
    "v_mfma_f32_16x16x32_bf16 v[92:95], v[148:151], v[136:139], v[92:95]\n"
    "v_mfma_f32_16x16x32_bf16 v[196:199], v[76:79], v[148:151], v[196:199]\n"
    "v_mfma_f32_16x16x32_bf16 v[88:91], v[144:147], v[136:139], v[88:91]\n"
    "v_mfma_f32_16x16x32_bf16 v[96:99], v[144:147], v[140:143], v[96:99]\n"
    "v_mfma_f32_16x16x32_bf16 v[196:199], v[72:75], v[144:147], v[196:199]\n"
    "s_waitcnt lgkmcnt(6)\n"
    "v_mfma_f32_16x16x32_bf16 v[18:21], v[176:179], v[160:163], v[18:21]\n"
    "buffer_load_dwordx4 v193, s[48:51], s84 offen offset:1024 lds\n"
    "s_add_u32 s93, s93, 1\n"
    "v_mfma_f32_16x16x32_bf16 v[26:29], v[176:179], v[164:167], v[26:29]\n"
    "s_waitcnt vmcnt(6) lgkmcnt(0)\n"
    "s_barrier\n"
    "v_mfma_f32_16x16x32_bf16 v[30:33], v[180:183], v[164:167], v[30:33]\n"
    "ds_read_b64_tr_b16 v[144:145], v52\n"
    "s_cmp_eq_u32 s93, 7\n"
    "v_mfma_f32_16x16x32_bf16 v[22:25], v[180:183], v[160:163], v[22:25]\n"
    "ds_read_b64_tr_b16 v[146:147], v52 offset:1024\n"
    "s_cselect_b32 s93, 0, s93\n"
    "v_mfma_f32_16x16x32_bf16 v[104:107], v[184:187], v[160:163], v[104:107]\n"
    "ds_read_b128 v[10:13], v48\n"
    "buffer_load_dwordx4 v194, s[48:51], s84 offen offset:2048 lds\n"
    "v_mfma_f32_16x16x32_bf16 v[112:115], v[184:187], v[164:167], v[112:115]\n"
    "ds_read_b128 v[34:37], v48 offset:2048\n"
    "v_cndmask_b32 v209, 0, v16, s[96:97]\n"
    "v_mfma_f32_16x16x32_bf16 v[116:119], v[188:191], v[164:167], v[116:119]\n"
    "ds_read_b64_tr_b16 v[148:149], v53\n"
    "v_cndmask_b32 v211, 0, v16, s[98:99]\n"
    "v_mfma_f32_16x16x32_bf16 v[108:111], v[188:191], v[160:163], v[108:111]\n"
    "ds_read_b64_tr_b16 v[150:151], v53 offset:1024\n"
    "v_mfma_f32_16x16x32_bf16 v[132:135], v[188:191], v[172:175], v[132:135]\n"
    "ds_read_b64_tr_b16 v[152:153], v54\n"
    "buffer_load_dwordx4 v195, s[48:51], s84 offen offset:3072 lds\n"
    "v_mfma_f32_16x16x32_bf16 v[124:127], v[188:191], v[168:171], v[124:127]\n"
    "ds_read_b64_tr_b16 v[154:155], v54 offset:1024\n"
    "v_cndmask_b32 v213, 0, v16, s[90:91]\n"
    "v_mfma_f32_16x16x32_bf16 v[120:123], v[184:187], v[168:171], v[120:123]\n"
    "ds_read_b64_tr_b16 v[156:157], v55\n"
    "v_cndmask_b32 v215, 0, v16, s[56:57]\n"
    "v_mfma_f32_16x16x32_bf16 v[128:131], v[184:187], v[172:175], v[128:131]\n"
    "ds_read_b64_tr_b16 v[158:159], v55 offset:1024\n"
    "v_mfma_f32_16x16x32_bf16 v[100:103], v[180:183], v[172:175], v[100:103]\n"
    "ds_read_b128 v[140:143], v48 offset:6144\n"
    "v_mov_b32 v14, v196\n"
    "v_mfma_f32_16x16x32_bf16 v[92:95], v[180:183], v[168:171], v[92:95]\n"
    "ds_read_b128 v[136:139], v48 offset:4096\n"
    "v_mfma_f32_16x16x32_bf16 v[88:91], v[176:179], v[168:171], v[88:91]\n"
    "s_mul_i32 s84, s93, 0x1c000\n"
    "s_add_u32 m0, s89, 0x10000\n"
    "v_mfma_f32_16x16x32_bf16 v[96:99], v[176:179], v[172:175], v[96:99]\n"
    "s_add_u32 s84, s84, s92\n"
    "buffer_load_dwordx4 v192, s[48:51], s84 offen lds\n"
    "s_waitcnt lgkmcnt(9)\n"
    "v_mfma_f32_16x16x32_bf16 v[18:21], v[144:147], v[10:13], v[18:21]\n"
    "ds_read_b64_tr_b16 v[176:177], v56 offset:8192\n"
    "ds_read_b64_tr_b16 v[178:179], v56 offset:9216\n"
    "s_waitcnt lgkmcnt(10)\n"
    "v_mfma_f32_16x16x32_bf16 v[26:29], v[144:147], v[34:37], v[26:29]\n"
    "ds_read_b128 v[160:163], v49\n"
    "ds_read_b128 v[164:167], v49 offset:2048\n"
    "v_mfma_f32_16x16x32_bf16 v[196:199], v[72:75], v[144:147], v[196:199]\n"
    "ds_read_b64_tr_b16 v[180:181], v57 offset:8192\n"
    "ds_read_b64_tr_b16 v[182:183], v57 offset:9216\n"
    "s_waitcnt lgkmcnt(12)\n"
    "v_mfma_f32_16x16x32_bf16 v[30:33], v[148:151], v[34:37], v[30:33]\n"
    "ds_read_b64_tr_b16 v[184:185], v58 offset:8192\n"
    "ds_read_b64_tr_b16 v[186:187], v58 offset:9216\n"
    "v_mfma_f32_16x16x32_bf16 v[22:25], v[148:151], v[10:13], v[22:25]\n"
    "ds_read_b64_tr_b16 v[188:189], v59 offset:8192\n"
    "ds_read_b64_tr_b16 v[190:191], v59 offset:9216\n"
    "v_mfma_f32_16x16x32_bf16 v[196:199], v[76:79], v[148:151], v[196:199]\n"
    "ds_read_b128 v[172:175], v49 offset:6144\n"
    "ds_read_b128 v[168:171], v49 offset:4096\n"
    "s_waitcnt lgkmcnt(14)\n"
    "v_mfma_f32_16x16x32_bf16 v[104:107], v[152:155], v[10:13], v[104:107]\n"
    "buffer_load_dwordx4 v193, s[48:51], s84 offen offset:1024 lds\n"
    "s_add_u32 s93, s93, 1\n"
    "v_mfma_f32_16x16x32_bf16 v[112:115], v[152:155], v[34:37], v[112:115]\n"
    "s_cmp_eq_u32 s93, 7\n"
    "s_cselect_b32 s93, 0, s93\n"
    "v_mfma_f32_16x16x32_bf16 v[196:199], v[80:83], v[152:155], v[196:199]\n"
    "ds_write_b32 v15, v14\n"
    "v_xor_b32 v15, 0x200, v15\n"
    "v_mfma_f32_16x16x32_bf16 v[116:119], v[156:159], v[34:37], v[116:119]\n"
    "buffer_load_dwordx4 v194, s[48:51], s84 offen offset:2048 lds\n"
    "v_mfma_f32_16x16x32_bf16 v[108:111], v[156:159], v[10:13], v[108:111]\n"
    "v_mfma_f32_16x16x32_bf16 v[196:199], v[84:87], v[156:159], v[196:199]\n"
    "s_waitcnt lgkmcnt(10)\n"
    "v_mfma_f32_16x16x32_bf16 v[18:21], v[176:179], v[160:163], v[18:21]\n"
    "s_waitcnt lgkmcnt(9)\n"
    "v_mfma_f32_16x16x32_bf16 v[26:29], v[176:179], v[164:167], v[26:29]\n"
    "v_mfma_f32_16x16x32_bf16 v[196:199], v[72:75], v[176:179], v[196:199]\n"
    "s_waitcnt lgkmcnt(7)\n"
    "v_mfma_f32_16x16x32_bf16 v[30:33], v[180:183], v[164:167], v[30:33]\n"
    "v_mfma_f32_16x16x32_bf16 v[22:25], v[180:183], v[160:163], v[22:25]\n"
    "v_mfma_f32_16x16x32_bf16 v[196:199], v[76:79], v[180:183], v[196:199]\n"
    "s_waitcnt lgkmcnt(5)\n"
    "v_mfma_f32_16x16x32_bf16 v[104:107], v[184:187], v[160:163], v[104:107]\n"
    "buffer_load_dwordx4 v195, s[48:51], s84 offen offset:3072 lds\n"
    "v_mfma_f32_16x16x32_bf16 v[112:115], v[184:187], v[164:167], v[112:115]\n"
    "v_mfma_f32_16x16x32_bf16 v[196:199], v[80:83], v[184:187], v[196:199]\n"
    "s_waitcnt vmcnt(8)\n"
    "s_waitcnt lgkmcnt(0)\n"
    "s_barrier\n"
    "ds_read_b32 v14, v15\n"
    "v_mfma_f32_16x16x32_bf16 v[116:119], v[188:191], v[164:167], v[116:119]\n"
    "v_mfma_f32_16x16x32_bf16 v[108:111], v[188:191], v[160:163], v[108:111]\n"
    "v_mfma_f32_16x16x32_bf16 v[196:199], v[84:87], v[188:191], v[196:199]\n"
    "v_mfma_f32_16x16x32_bf16 v[132:135], v[156:159], v[140:143], v[132:135]\n"
    "s_lshl_b32 s75, s69, 2\n"
    "s_add_u32 s75, s75, 0x27f00\n"
    "v_mfma_f32_16x16x32_bf16 v[124:127], v[156:159], v[136:139], v[124:127]\n"
    "v_mov_b32 v71, s75\n"
    "s_xor_b32 s75, s75, 4\n"
    "v_mfma_f32_16x16x32_bf16 v[120:123], v[152:155], v[136:139], v[120:123]\n"
    "v_mov_b32 v220, s75\n"
    "v_mov_b32 v221, 1\n"
    "v_mfma_f32_16x16x32_bf16 v[128:131], v[152:155], v[140:143], v[128:131]\n"
    "s_waitcnt lgkmcnt(0)\n"
    "v_add_f32 v14, v14, v196\n"
    "v_cvt_pk_bf16_f32 v3, v14, v14\n"
    "v_mfma_f32_16x16x32_bf16 v[100:103], v[148:151], v[140:143], v[100:103]\n"
    "v_lshlrev_b32 v3, 16, v3\n"
    "v_sub_f32 v4, v14, v3\n"
    "v_mfma_f32_16x16x32_bf16 v[92:95], v[148:151], v[136:139], v[92:95]\n"
    "v_cvt_pk_bf16_f32 v14, v3, v4\n"
    "v_cndmask_b32 v208, 0, v14, s[96:97]\n"
    "v_mfma_f32_16x16x32_bf16 v[88:91], v[144:147], v[136:139], v[88:91]\n"
    "v_cndmask_b32 v210, 0, v14, s[98:99]\n"
    "v_cndmask_b32 v212, 0, v14, s[90:91]\n"
    "v_mfma_f32_16x16x32_bf16 v[96:99], v[144:147], v[140:143], v[96:99]\n"
    "v_cndmask_b32 v214, 0, v14, s[56:57]\n"
    "v_mfma_f32_16x16x16_bf16 v[18:21], v[208:209], v[200:201], v[18:21]\n"
    "v_min_u32 v39, 6, v1\n"
    "v_mfma_f32_16x16x16_bf16 v[26:29], v[208:209], v[202:203], v[26:29]\n"
    "v_mfma_f32_16x16x16_bf16 v[22:25], v[210:211], v[200:201], v[22:25]\n"
    "v_lshlrev_b32 v39, 7, v39\n"
    "v_mfma_f32_16x16x16_bf16 v[30:33], v[210:211], v[202:203], v[30:33]\n"
    "v_mfma_f32_16x16x16_bf16 v[104:107], v[212:213], v[200:201], v[104:107]\n"
    "s_lshl_b32 s75, s79, 7\n"
    "v_mfma_f32_16x16x16_bf16 v[112:115], v[212:213], v[202:203], v[112:115]\n"
    "v_mfma_f32_16x16x16_bf16 v[108:111], v[214:215], v[200:201], v[108:111]\n"
    "s_add_u32 s72, s75, s70\n"
    "v_mfma_f32_16x16x16_bf16 v[116:119], v[214:215], v[202:203], v[116:119]\n"
    "v_mfma_f32_16x16x32_bf16 v[132:135], v[188:191], v[172:175], v[132:135]\n"
    "s_lshl_b32 s74, s79, 12\n"
    "s_add_u32 s74, s74, s88\n"
    "v_mul_f32 v18, v18, |v18|\n"
    "v_mfma_f32_16x16x32_bf16 v[124:127], v[188:191], v[168:171], v[124:127]\n"
    "v_mul_f32 v19, v19, |v19|\n"
    "v_cvt_pk_bf16_f32 v18, v18, v19\n"
    "v_mfma_f32_16x16x32_bf16 v[120:123], v[184:187], v[168:171], v[120:123]\n"
    "v_pk_max_i16 v18, v18, 0\n"
    "v_mul_f32 v20, v20, |v20|\n"
    "v_mfma_f32_16x16x32_bf16 v[128:131], v[184:187], v[172:175], v[128:131]\n"
    "v_mul_f32 v21, v21, |v21|\n"
    "v_cvt_pk_bf16_f32 v19, v20, v21\n"
    "v_mfma_f32_16x16x32_bf16 v[100:103], v[180:183], v[172:175], v[100:103]\n"
    "v_pk_max_i16 v19, v19, 0\n"
    "v_mul_f32 v22, v22, |v22|\n"
    "v_mfma_f32_16x16x32_bf16 v[92:95], v[180:183], v[168:171], v[92:95]\n"
    "v_mul_f32 v23, v23, |v23|\n"
    "v_cvt_pk_bf16_f32 v20, v22, v23\n"
    "v_mfma_f32_16x16x32_bf16 v[88:91], v[176:179], v[168:171], v[88:91]\n"
    "v_pk_max_i16 v20, v20, 0\n"
    "v_mul_f32 v24, v24, |v24|\n"
    "v_mfma_f32_16x16x32_bf16 v[96:99], v[176:179], v[172:175], v[96:99]\n"
    "v_mul_f32 v25, v25, |v25|\n"
    "s_nop 1\n"
    "v_mfma_f32_16x16x16_bf16 v[88:91], v[208:209], v[204:205], v[88:91]\n"
    "v_cvt_pk_bf16_f32 v21, v24, v25\n"
    "v_pk_max_i16 v21, v21, 0\n"
    "v_mfma_f32_16x16x16_bf16 v[96:99], v[208:209], v[206:207], v[96:99]\n"
    "v_mul_f32 v26, v26, |v26|\n"
    "v_mul_f32 v27, v27, |v27|\n"
    "v_mfma_f32_16x16x16_bf16 v[92:95], v[210:211], v[204:205], v[92:95]\n"
    "v_cvt_pk_bf16_f32 v22, v26, v27\n"
    "v_pk_max_i16 v22, v22, 0\n"
    "v_mfma_f32_16x16x16_bf16 v[100:103], v[210:211], v[206:207], v[100:103]\n"
    "v_mul_f32 v28, v28, |v28|\n"
    "v_mul_f32 v29, v29, |v29|\n"
    "v_mfma_f32_16x16x16_bf16 v[120:123], v[212:213], v[204:205], v[120:123]\n"
    "v_cvt_pk_bf16_f32 v23, v28, v29\n"
    "v_pk_max_i16 v23, v23, 0\n"
    "v_mfma_f32_16x16x16_bf16 v[128:131], v[212:213], v[206:207], v[128:131]\n"
    "v_mul_f32 v30, v30, |v30|\n"
    "v_mul_f32 v31, v31, |v31|\n"
    "v_mfma_f32_16x16x16_bf16 v[124:127], v[214:215], v[204:205], v[124:127]\n"
    "v_cvt_pk_bf16_f32 v24, v30, v31\n"
    "v_pk_max_i16 v24, v24, 0\n"
    "v_mfma_f32_16x16x16_bf16 v[132:135], v[214:215], v[206:207], v[132:135]\n"
    "v_mul_f32 v32, v32, |v32|\n"
    "v_mul_f32 v33, v33, |v33|\n"
    "v_cvt_pk_bf16_f32 v25, v32, v33\n"
    "v_pk_max_i16 v25, v25, 0\n"
    "v_permlane16_swap_b32 v18, v22\n"
    "v_permlane16_swap_b32 v19, v23\n"
    "v_permlane16_swap_b32 v20, v24\n"
    "v_permlane16_swap_b32 v21, v25\n"
    "buffer_store_dwordx4 v[18:21], v2, s[52:55], s74 offen sc1\n"
    "buffer_store_dwordx4 v[22:25], v2, s[52:55], s74 offen offset:1024 sc1\n"
    "v_mul_f32 v104, v104, |v104|\n"
    "v_mul_f32 v105, v105, |v105|\n"
    "v_cvt_pk_bf16_f32 v104, v104, v105\n"
    "v_pk_max_i16 v104, v104, 0\n"
    "v_mul_f32 v106, v106, |v106|\n"
    "v_mul_f32 v107, v107, |v107|\n"
    "v_cvt_pk_bf16_f32 v105, v106, v107\n"
    "v_pk_max_i16 v105, v105, 0\n"
    "v_mul_f32 v108, v108, |v108|\n"
    "v_mul_f32 v109, v109, |v109|\n"
    "v_cvt_pk_bf16_f32 v106, v108, v109\n"
    "v_pk_max_i16 v106, v106, 0\n"
    "v_mul_f32 v110, v110, |v110|\n"
    "v_mul_f32 v111, v111, |v111|\n"
    "v_cvt_pk_bf16_f32 v107, v110, v111\n"
    "v_pk_max_i16 v107, v107, 0\n"
    "v_mul_f32 v112, v112, |v112|\n"
    "v_mul_f32 v113, v113, |v113|\n"
    "v_cvt_pk_bf16_f32 v108, v112, v113\n"
    "v_pk_max_i16 v108, v108, 0\n"
    "v_mul_f32 v114, v114, |v114|\n"
    "v_mul_f32 v115, v115, |v115|\n"
    "v_cvt_pk_bf16_f32 v109, v114, v115\n"
    "v_pk_max_i16 v109, v109, 0\n"
    "v_mul_f32 v116, v116, |v116|\n"
    "v_mul_f32 v117, v117, |v117|\n"
    "v_cvt_pk_bf16_f32 v110, v116, v117\n"
    "v_pk_max_i16 v110, v110, 0\n"
    "v_mul_f32 v118, v118, |v118|\n"
    "v_mul_f32 v119, v119, |v119|\n"
    "v_cvt_pk_bf16_f32 v111, v118, v119\n"
    "v_pk_max_i16 v111, v111, 0\n"
    "v_permlane16_swap_b32 v104, v108\n"
    "v_permlane16_swap_b32 v105, v109\n"
    "v_permlane16_swap_b32 v106, v110\n"
    "v_permlane16_swap_b32 v107, v111\n"
    "buffer_store_dwordx4 v[104:107], v2, s[52:55], s74 offen offset:2048 sc1\n"
    "buffer_store_dwordx4 v[108:111], v2, s[52:55], s74 offen offset:3072 sc1\n"
    "s_add_u32 s74, s74, 0x7000\n"
    "v_mul_f32 v88, v88, |v88|\n"
    "v_mul_f32 v89, v89, |v89|\n"
    "v_cvt_pk_bf16_f32 v88, v88, v89\n"
    "v_pk_max_i16 v88, v88, 0\n"
    "v_mul_f32 v90, v90, |v90|\n"
    "v_mul_f32 v91, v91, |v91|\n"
    "v_cvt_pk_bf16_f32 v89, v90, v91\n"
    "v_pk_max_i16 v89, v89, 0\n"
    "v_mul_f32 v92, v92, |v92|\n"
    "v_mul_f32 v93, v93, |v93|\n"
    "v_cvt_pk_bf16_f32 v90, v92, v93\n"
    "v_pk_max_i16 v90, v90, 0\n"
    "v_mul_f32 v94, v94, |v94|\n"
    "v_mul_f32 v95, v95, |v95|\n"
    "v_cvt_pk_bf16_f32 v91, v94, v95\n"
    "v_pk_max_i16 v91, v91, 0\n"
    "v_mul_f32 v96, v96, |v96|\n"
    "v_mul_f32 v97, v97, |v97|\n"
    "v_cvt_pk_bf16_f32 v92, v96, v97\n"
    "v_pk_max_i16 v92, v92, 0\n"
    "v_mul_f32 v98, v98, |v98|\n"
    "v_mul_f32 v99, v99, |v99|\n"
    "v_cvt_pk_bf16_f32 v93, v98, v99\n"
    "v_pk_max_i16 v93, v93, 0\n"
    "v_mul_f32 v100, v100, |v100|\n"
    "v_mul_f32 v101, v101, |v101|\n"
    "v_cvt_pk_bf16_f32 v94, v100, v101\n"
    "v_pk_max_i16 v94, v94, 0\n"
    "v_mul_f32 v102, v102, |v102|\n"
    "v_mul_f32 v103, v103, |v103|\n"
    "v_cvt_pk_bf16_f32 v95, v102, v103\n"
    "v_pk_max_i16 v95, v95, 0\n"
    "v_permlane16_swap_b32 v88, v92\n"
    "v_permlane16_swap_b32 v89, v93\n"
    "v_permlane16_swap_b32 v90, v94\n"
    "v_permlane16_swap_b32 v91, v95\n"
    "v_mul_f32 v120, v120, |v120|\n"
    "v_mul_f32 v121, v121, |v121|\n"
    "v_cvt_pk_bf16_f32 v120, v120, v121\n"
    "v_pk_max_i16 v120, v120, 0\n"
    "v_mul_f32 v122, v122, |v122|\n"
    "v_mul_f32 v123, v123, |v123|\n"
    "v_cvt_pk_bf16_f32 v121, v122, v123\n"
    "v_pk_max_i16 v121, v121, 0\n"
    "v_mul_f32 v124, v124, |v124|\n"
    "v_mul_f32 v125, v125, |v125|\n"
    "v_cvt_pk_bf16_f32 v122, v124, v125\n"
    "v_pk_max_i16 v122, v122, 0\n"
    "v_mul_f32 v126, v126, |v126|\n"
    "v_mul_f32 v127, v127, |v127|\n"
    "v_cvt_pk_bf16_f32 v123, v126, v127\n"
    "v_pk_max_i16 v123, v123, 0\n"
    "v_mul_f32 v128, v128, |v128|\n"
    "v_mul_f32 v129, v129, |v129|\n"
    "v_cvt_pk_bf16_f32 v124, v128, v129\n"
    "v_pk_max_i16 v124, v124, 0\n"
    "v_mul_f32 v130, v130, |v130|\n"
    "v_mul_f32 v131, v131, |v131|\n"
    "v_cvt_pk_bf16_f32 v125, v130, v131\n"
    "v_pk_max_i16 v125, v125, 0\n"
    "v_mul_f32 v132, v132, |v132|\n"
    "v_mul_f32 v133, v133, |v133|\n"
    "v_cvt_pk_bf16_f32 v126, v132, v133\n"
    "v_pk_max_i16 v126, v126, 0\n"
    "v_mul_f32 v134, v134, |v134|\n"
    "v_mul_f32 v135, v135, |v135|\n"
    "v_cvt_pk_bf16_f32 v127, v134, v135\n"
    "v_pk_max_i16 v127, v127, 0\n"
    "v_permlane16_swap_b32 v120, v124\n"
    "v_permlane16_swap_b32 v121, v125\n"
    "v_permlane16_swap_b32 v122, v126\n"
    "v_permlane16_swap_b32 v123, v127\n"
    "s_setprio 2\n"
    "ds_read_b64_tr_b16 v[26:27], v216 offset:32768\n"
    "ds_read_b64_tr_b16 v[28:29], v216 offset:34816\n"
    "ds_read_b64_tr_b16 v[30:31], v217 offset:32768\n"
    "ds_read_b64_tr_b16 v[32:33], v217 offset:34816\n"
    "ds_read_b64_tr_b16 v[56:57], v216 offset:33280\n"
    "ds_read_b64_tr_b16 v[58:59], v216 offset:35328\n"
    "ds_read_b64_tr_b16 v[60:61], v217 offset:33280\n"
    "ds_read_b64_tr_b16 v[62:63], v217 offset:35328\n"
    "ds_read_b64_tr_b16 v[96:97], v216 offset:36864\n"
    "ds_read_b64_tr_b16 v[98:99], v216 offset:38912\n"
    "ds_read_b64_tr_b16 v[100:101], v217 offset:36864\n"
    "ds_read_b64_tr_b16 v[102:103], v217 offset:38912\n"
    "ds_read_b64_tr_b16 v[112:113], v216 offset:37376\n"
    "ds_read_b64_tr_b16 v[114:115], v216 offset:39424\n"
    "ds_read_b64_tr_b16 v[116:117], v217 offset:37376\n"
    "ds_read_b64_tr_b16 v[118:119], v217 offset:39424\n"
    "s_waitcnt vmcnt(0)\n"
    "s_mov_b64 exec, 1\n"
    "buffer_store_dwordx2 v[8:9], off, s[52:55], s72 sc1\n"
    "s_mov_b64 exec, -1\n"
    "buffer_store_dwordx4 v[88:91], v2, s[52:55], s74 offen sc1\n"
    "buffer_store_dwordx4 v[92:95], v2, s[52:55], s74 offen offset:1024 sc1\n"
    "buffer_store_dwordx4 v[120:123], v2, s[52:55], s74 offen offset:2048 sc1\n"
    "buffer_store_dwordx4 v[124:127], v2, s[52:55], s74 offen offset:3072 sc1\n"
    "s_waitcnt lgkmcnt(14)\n"
    "v_mfma_f32_32x32x16_bf16 v[40:55], v[26:29], v[18:21], 0\n"
    "s_mul_i32 s84, s93, 0x1c000\n"
    "s_add_u32 m0, s89, 0x14000\n"
    "s_add_u32 s84, s84, s92\n"
    "buffer_load_dwordx4 v192, s[48:51], s84 offen lds\n"
    "s_waitcnt lgkmcnt(12)\n"
    "v_mfma_f32_32x32x16_bf16 v[72:87], v[30:33], v[18:21], 0\n"
    "v_mfma_f32_32x32x16_bf16 v[136:151], v[26:29], v[88:91], 0\n"
    "buffer_load_dwordx4 v193, s[48:51], s84 offen offset:1024 lds\n"
    "s_add_u32 s93, s93, 1\n"
    "s_cmp_eq_u32 s93, 7\n"
    "s_cselect_b32 s93, 0, s93\n"
    "v_mfma_f32_32x32x16_bf16 v[152:167], v[30:33], v[88:91], 0\n"
    "s_waitcnt lgkmcnt(10)\n"
    "v_mfma_f32_32x32x16_bf16 v[40:55], v[56:59], v[22:25], v[40:55]\n"
    "buffer_load_dwordx4 v194, s[48:51], s84 offen offset:2048 lds\n"
    "s_waitcnt lgkmcnt(8)\n"
    "v_mfma_f32_32x32x16_bf16 v[72:87], v[60:63], v[22:25], v[72:87]\n"
    "v_mfma_f32_32x32x16_bf16 v[136:151], v[56:59], v[92:95], v[136:151]\n"
    "buffer_load_dwordx4 v195, s[48:51], s84 offen offset:3072 lds\n"
    "v_mfma_f32_32x32x16_bf16 v[152:167], v[60:63], v[92:95], v[152:167]\n"
    "s_waitcnt lgkmcnt(6)\n"
    "v_mfma_f32_32x32x16_bf16 v[40:55], v[96:99], v[104:107], v[40:55]\n"
    "s_mul_i32 s84, s93, 0x1c000\n"
    "s_add_u32 m0, s89, 0\n"
    "s_add_u32 s84, s84, s92\n"
    "buffer_load_dwordx4 v192, s[48:51], s84 offen lds\n"
    "s_waitcnt lgkmcnt(4)\n"
    "v_mfma_f32_32x32x16_bf16 v[72:87], v[100:103], v[104:107], v[72:87]\n"
    "v_mfma_f32_32x32x16_bf16 v[136:151], v[96:99], v[120:123], v[136:151]\n"
    "buffer_load_dwordx4 v193, s[48:51], s84 offen offset:1024 lds\n"
    "s_add_u32 s93, s93, 1\n"
    "s_cmp_eq_u32 s93, 7\n"
    "s_cselect_b32 s93, 0, s93\n"
    "v_mfma_f32_32x32x16_bf16 v[152:167], v[100:103], v[120:123], v[152:167]\n"
    "s_waitcnt lgkmcnt(2)\n"
    "v_mfma_f32_32x32x16_bf16 v[40:55], v[112:115], v[108:111], v[40:55]\n"
    "buffer_load_dwordx4 v194, s[48:51], s84 offen offset:2048 lds\n"
    "s_waitcnt lgkmcnt(0)\n"
    "v_mfma_f32_32x32x16_bf16 v[72:87], v[116:119], v[108:111], v[72:87]\n"
    "v_mfma_f32_32x32x16_bf16 v[136:151], v[112:115], v[124:127], v[136:151]\n"
    "buffer_load_dwordx4 v195, s[48:51], s84 offen offset:3072 lds\n"
    "v_mfma_f32_32x32x16_bf16 v[152:167], v[116:119], v[124:127], v[152:167]\n"
    "v_and_b32 v3, 7, v68\n"
    "v_xor_b32 v3, v3, v17\n"
    "v_lshlrev_b32 v3, 4, v3\n"
    "v_lshl_add_u32 v3, v68, 8, v3\n"
    "s_and_b32 s74, s69, 1\n"
    "s_mul_i32 s74, s74, 0xa600\n"
    "s_lshr_b32 s75, s69, 1\n"
    "s_lshl_b32 s75, s75, 13\n"
    "s_add_u32 s74, s74, s75\n"
    "s_add_u32 s74, s74, 0x18000\n"
    "v_add_u32 v10, s74, v3\n"
    "v_xor_b32 v11, 32, v10\n"
    "v_xor_b32 v12, 64, v10\n"
    "v_xor_b32 v13, 0x60, v10\n"
    "buffer_load_dwordx2 v[222:223], v39, s[52:55], s70 offen sc1\n"
    "s_add_u32 s72, s72, 0x1000\n"
    "s_waitcnt vmcnt(9)\n"
    "s_mov_b64 exec, 1\n"
    "buffer_store_dwordx2 v[8:9], off, s[52:55], s72 sc1\n"
    "s_mov_b64 exec, -1\n"
    "s_mul_i32 s84, s93, 0x1c000\n"
    "s_add_u32 m0, s89, 0x4000\n"
    "s_add_u32 s84, s84, s92\n"
    "buffer_load_dwordx4 v192, s[48:51], s84 offen lds\n"
    "buffer_load_dwordx4 v193, s[48:51], s84 offen offset:1024 lds\n"
    "s_add_u32 s93, s93, 1\n"
    "s_cmp_eq_u32 s93, 7\n"
    "s_cselect_b32 s93, 0, s93\n"
    "buffer_load_dwordx4 v194, s[48:51], s84 offen offset:2048 lds\n"
    "buffer_load_dwordx4 v195, s[48:51], s84 offen offset:3072 lds\n"
    "v_and_b32 v6, 3, v1\n"
    "v_lshlrev_b32 v6, 2, v6\n"
    "v_add_u32 v6, 0x27f00, v6\n"
    "ds_read_b32 v5, v6 offset:112\n"
    "s_waitcnt lgkmcnt(0)\n"
    "v_cmp_eq_u32 vcc, 0, v5\n"
    "s_nop 0\n"
    "s_cbranch_vccnz .Lslows6%=\n"
    ".Loks6%=:\n"
    "s_mul_i32 s84, s93, 0x1c000\n"
    "s_add_u32 m0, s89, 0x1c100\n"
    "s_add_u32 s84, s84, s92\n"
    "buffer_load_dwordx4 v192, s[48:51], s84 offen lds\n"
    "buffer_load_dwordx4 v193, s[48:51], s84 offen offset:1024 lds\n"
    "s_add_u32 s93, s93, 1\n"
    "s_cmp_eq_u32 s93, 7\n"
    "s_cselect_b32 s93, 0, s93\n"
    "buffer_load_dwordx4 v194, s[48:51], s84 offen offset:2048 lds\n"
    "buffer_load_dwordx4 v195, s[48:51], s84 offen offset:3072 lds\n"
    "s_waitcnt vmcnt(9)\n"
    "v_cmp_ne_u64 vcc, s[76:77], v[222:223]\n"
    "s_nop 0\n"
    "s_cbranch_vccnz .Lslowh%=\n"
    ".Lokh%=:\n"
    "s_add_u32 s70, s70, 0x1000\n"
    "s_mov_b32 s93, s79\n"
    "s_add_u32 s93, s93, 1\n"
    "s_cmp_eq_u32 s93, 7\n"
    "s_cselect_b32 s93, 0, s93\n"
    "s_lshl_b32 s73, s93, 12\n"
    "s_add_u32 s73, s73, s88\n"
    "buffer_load_dwordx4 v[18:21], v2, s[52:55], s73 offen sc1\n"
    "buffer_load_dwordx4 v[22:25], v2, s[52:55], s73 offen offset:1024 sc1\n"
    "buffer_load_dwordx4 v[34:37], v2, s[52:55], s73 offen offset:2048 sc1\n"
    "buffer_load_dwordx4 v[64:67], v2, s[52:55], s73 offen offset:3072 sc1\n"
    "s_add_u32 s93, s93, 1\n"
    "s_cmp_eq_u32 s93, 7\n"
    "s_cselect_b32 s93, 0, s93\n"
    "s_lshl_b32 s73, s93, 12\n"
    "s_add_u32 s73, s73, s88\n"
    "buffer_load_dwordx4 v[88:91], v2, s[52:55], s73 offen sc1\n"
    "buffer_load_dwordx4 v[92:95], v2, s[52:55], s73 offen offset:1024 sc1\n"
    "s_waitcnt vmcnt(6)\n"
    "s_mov_b64 exec, 1\n"
    "ds_write_b32 v71, v221 offset:16\n"
    "s_mov_b64 exec, -1\n"
    "ds_read_b32 v3, v220 offset:16\n"
    "s_waitcnt lgkmcnt(0)\n"
    "v_cmp_eq_u32 vcc, 0, v3\n"
    "s_nop 0\n"
    "s_cbranch_vccnz .Lslows1%=\n"
    ".Loks1%=:\n"
    "ds_read_b64_tr_b16 v[26:27], v216 offset:49152\n"
    "ds_read_b64_tr_b16 v[28:29], v216 offset:51200\n"
    "ds_read_b64_tr_b16 v[30:31], v217 offset:49152\n"
    "ds_read_b64_tr_b16 v[32:33], v217 offset:51200\n"
    "ds_read_b64_tr_b16 v[56:57], v216 offset:49664\n"
    "ds_read_b64_tr_b16 v[58:59], v216 offset:51712\n"
    "ds_read_b64_tr_b16 v[60:61], v217 offset:49664\n"
    "ds_read_b64_tr_b16 v[62:63], v217 offset:51712\n"
    "ds_read_b64_tr_b16 v[96:97], v216 offset:53248\n"
    "ds_read_b64_tr_b16 v[98:99], v216 offset:55296\n"
    "ds_read_b64_tr_b16 v[100:101], v217 offset:53248\n"
    "ds_read_b64_tr_b16 v[102:103], v217 offset:55296\n"
    "buffer_load_dwordx4 v[104:107], v2, s[52:55], s73 offen offset:2048 sc1\n"
    "buffer_load_dwordx4 v[108:111], v2, s[52:55], s73 offen offset:3072 sc1\n"
    "s_add_u32 s93, s93, 1\n"
    "s_cmp_eq_u32 s93, 7\n"
    "s_cselect_b32 s93, 0, s93\n"
    "s_lshl_b32 s73, s93, 12\n"
    "s_add_u32 s73, s73, s88\n"
    "buffer_load_dwordx4 v[120:123], v2, s[52:55], s73 offen sc1\n"
    "buffer_load_dwordx4 v[124:127], v2, s[52:55], s73 offen offset:1024 sc1\n"
    "buffer_load_dwordx4 v[128:131], v2, s[52:55], s73 offen offset:2048 sc1\n"
    "buffer_load_dwordx4 v[132:135], v2, s[52:55], s73 offen offset:3072 sc1\n"
    "s_add_u32 s93, s93, 1\n"
    "s_cmp_eq_u32 s93, 7\n"
    "s_cselect_b32 s93, 0, s93\n"
    "s_lshl_b32 s73, s93, 12\n"
    "s_add_u32 s73, s73, s88\n"
    "buffer_load_dwordx4 v[168:171], v2, s[52:55], s73 offen sc1\n"
    "buffer_load_dwordx4 v[172:175], v2, s[52:55], s73 offen offset:1024 sc1\n"
    "s_setprio 0\n"
    "s_waitcnt vmcnt(13) lgkmcnt(10)\n"
    "v_mfma_f32_32x32x16_bf16 v[40:55], v[26:29], v[18:21], v[40:55]\n"
    "ds_read_b64_tr_b16 v[112:113], v216 offset:53760\n"
    "ds_read_b64_tr_b16 v[114:115], v216 offset:55808\n"
    "s_waitcnt lgkmcnt(10)\n"
    "v_mfma_f32_32x32x16_bf16 v[72:87], v[30:33], v[18:21], v[72:87]\n"
    "ds_read_b64_tr_b16 v[116:117], v217 offset:53760\n"
    "ds_read_b64_tr_b16 v[118:119], v217 offset:55808\n"
    "buffer_load_dwordx4 v[18:21], v2, s[52:55], s73 offen offset:2048 sc1\n"
    "s_waitcnt vmcnt(13) lgkmcnt(10)\n"
    "v_mfma_f32_32x32x16_bf16 v[40:55], v[56:59], v[22:25], v[40:55]\n"
    "ds_read_b64_tr_b16 v[26:27], v218\n"
    "ds_read_b64_tr_b16 v[28:29], v218 offset:2048\n"
    "s_waitcnt lgkmcnt(10)\n"
    "v_mfma_f32_32x32x16_bf16 v[72:87], v[60:63], v[22:25], v[72:87]\n"
    "ds_read_b64_tr_b16 v[30:31], v219\n"
    "ds_read_b64_tr_b16 v[32:33], v219 offset:2048\n"
    "buffer_load_dwordx4 v[22:25], v2, s[52:55], s73 offen offset:3072 sc1\n"
    "s_waitcnt vmcnt(13) lgkmcnt(10)\n"
    "v_mfma_f32_32x32x16_bf16 v[40:55], v[96:99], v[34:37], v[40:55]\n"
    "ds_read_b64_tr_b16 v[56:57], v218 offset:512\n"
    "ds_read_b64_tr_b16 v[58:59], v218 offset:2560\n"
    "buffer_load_dwordx2 v[222:223], v39, s[52:55], s70 offen sc1\n"
    "s_waitcnt lgkmcnt(10)\n"
    "v_mfma_f32_32x32x16_bf16 v[72:87], v[100:103], v[34:37], v[72:87]\n"
    "ds_read_b64_tr_b16 v[60:61], v219 offset:512\n"
    "ds_read_b64_tr_b16 v[62:63], v219 offset:2560\n"
    "s_add_u32 s93, s93, 1\n"
    "s_cmp_eq_u32 s93, 7\n"
    "s_cselect_b32 s93, 0, s93\n"
    "s_lshl_b32 s73, s93, 12\n"
    "s_add_u32 s73, s73, s88\n"
    "buffer_load_dwordx4 v[34:37], v2, s[52:55], s73 offen sc1\n"
    "s_waitcnt vmcnt(14) lgkmcnt(10)\n"
    "v_mfma_f32_32x32x16_bf16 v[40:55], v[112:115], v[64:67], v[40:55]\n"
    "ds_read_b64_tr_b16 v[96:97], v218 offset:4096\n"
    "ds_read_b64_tr_b16 v[98:99], v218 offset:6144\n"
    "s_waitcnt lgkmcnt(10)\n"
    "v_mfma_f32_32x32x16_bf16 v[72:87], v[116:119], v[64:67], v[72:87]\n"
    "ds_read_b64_tr_b16 v[100:101], v219 offset:4096\n"
    "ds_read_b64_tr_b16 v[102:103], v219 offset:6144\n"
    "buffer_load_dwordx4 v[64:67], v2, s[52:55], s73 offen offset:1024 sc1\n"
    "s_waitcnt vmcnt(14) lgkmcnt(10)\n"
    "v_mfma_f32_32x32x16_bf16 v[40:55], v[26:29], v[88:91], v[40:55]\n"
    "ds_read_b64_tr_b16 v[112:113], v218 offset:4608\n"
    "ds_read_b64_tr_b16 v[114:115], v218 offset:6656\n"
    "s_waitcnt lgkmcnt(10)\n"
    "v_mfma_f32_32x32x16_bf16 v[72:87], v[30:33], v[88:91], v[72:87]\n"
    "ds_read_b64_tr_b16 v[116:117], v219 offset:4608\n"
    "ds_read_b64_tr_b16 v[118:119], v219 offset:6656\n"
    "buffer_load_dwordx4 v[88:91], v2, s[52:55], s73 offen offset:2048 sc1\n"
    "s_waitcnt vmcnt(14) lgkmcnt(10)\n"
    "v_mfma_f32_32x32x16_bf16 v[40:55], v[56:59], v[92:95], v[40:55]\n"
    "ds_read_b64_tr_b16 v[26:27], v218 offset:16384\n"
    "ds_read_b64_tr_b16 v[28:29], v218 offset:18432\n"
    "s_waitcnt lgkmcnt(10)\n"
    "v_mfma_f32_32x32x16_bf16 v[72:87], v[60:63], v[92:95], v[72:87]\n"
    "ds_read_b64_tr_b16 v[30:31], v219 offset:16384\n"
    "ds_read_b64_tr_b16 v[32:33], v219 offset:18432\n"
    "buffer_load_dwordx4 v[92:95], v2, s[52:55], s73 offen offset:3072 sc1\n"
    "s_waitcnt vmcnt(14) lgkmcnt(10)\n"
    "v_mfma_f32_32x32x16_bf16 v[40:55], v[96:99], v[104:107], v[40:55]\n"
    "ds_read_b64_tr_b16 v[56:57], v218 offset:16896\n"
    "ds_read_b64_tr_b16 v[58:59], v218 offset:18944\n"
    "s_waitcnt lgkmcnt(10)\n"
    "v_mfma_f32_32x32x16_bf16 v[72:87], v[100:103], v[104:107], v[72:87]\n"
    "ds_read_b64_tr_b16 v[60:61], v219 offset:16896\n"
    "ds_read_b64_tr_b16 v[62:63], v219 offset:18944\n"
    "s_add_u32 s93, s93, 1\n"
    "s_cmp_eq_u32 s93, 7\n"
    "s_cselect_b32 s93, 0, s93\n"
    "s_lshl_b32 s73, s93, 12\n"
    "s_add_u32 s73, s73, s88\n"
    "buffer_load_dwordx4 v[104:107], v2, s[52:55], s73 offen sc1\n"
    "s_waitcnt vmcnt(14) lgkmcnt(10)\n"
    "v_mfma_f32_32x32x16_bf16 v[40:55], v[112:115], v[108:111], v[40:55]\n"
    "ds_read_b64_tr_b16 v[96:97], v218 offset:20480\n"
    "ds_read_b64_tr_b16 v[98:99], v218 offset:22528\n"
    "s_waitcnt lgkmcnt(10)\n"
    "v_mfma_f32_32x32x16_bf16 v[72:87], v[116:119], v[108:111], v[72:87]\n"
    "ds_read_b64_tr_b16 v[100:101], v219 offset:20480\n"
    "ds_read_b64_tr_b16 v[102:103], v219 offset:22528\n"
    "buffer_load_dwordx4 v[108:111], v2, s[52:55], s73 offen offset:1024 sc1\n"
    "s_waitcnt vmcnt(14) lgkmcnt(10)\n"
    "v_mfma_f32_32x32x16_bf16 v[40:55], v[26:29], v[120:123], v[40:55]\n"
    "ds_read_b64_tr_b16 v[112:113], v218 offset:20992\n"
    "ds_read_b64_tr_b16 v[114:115], v218 offset:23040\n"
    "s_waitcnt lgkmcnt(10)\n"
    "v_mfma_f32_32x32x16_bf16 v[72:87], v[30:33], v[120:123], v[72:87]\n"
    "ds_read_b64_tr_b16 v[116:117], v219 offset:20992\n"
    "ds_read_b64_tr_b16 v[118:119], v219 offset:23040\n"
    "buffer_load_dwordx4 v[120:123], v2, s[52:55], s73 offen offset:2048 sc1\n"
    "s_waitcnt vmcnt(14) lgkmcnt(10)\n"
    "v_mfma_f32_32x32x16_bf16 v[40:55], v[56:59], v[124:127], v[40:55]\n"
    "ds_read_b64_tr_b16 v[26:27], v216\n"
    "ds_read_b64_tr_b16 v[28:29], v216 offset:2048\n"
    "s_waitcnt lgkmcnt(10)\n"
    "v_mfma_f32_32x32x16_bf16 v[72:87], v[60:63], v[124:127], v[72:87]\n"
    "ds_read_b64_tr_b16 v[30:31], v217\n"
    "ds_read_b64_tr_b16 v[32:33], v217 offset:2048\n"
    "buffer_load_dwordx4 v[124:127], v2, s[52:55], s73 offen offset:3072 sc1\n"
    "s_waitcnt vmcnt(14) lgkmcnt(10)\n"
    "v_mfma_f32_32x32x16_bf16 v[40:55], v[96:99], v[128:131], v[40:55]\n"
    "ds_read_b64_tr_b16 v[56:57], v216 offset:512\n"
    "ds_read_b64_tr_b16 v[58:59], v216 offset:2560\n"
    "s_waitcnt lgkmcnt(10)\n"
    "v_mfma_f32_32x32x16_bf16 v[72:87], v[100:103], v[128:131], v[72:87]\n"
    "ds_read_b64_tr_b16 v[60:61], v217 offset:512\n"
    "ds_read_b64_tr_b16 v[62:63], v217 offset:2560\n"
    "s_waitcnt vmcnt(13) lgkmcnt(10)\n"
    "v_mfma_f32_32x32x16_bf16 v[40:55], v[112:115], v[132:135], v[40:55]\n"
    "ds_read_b64_tr_b16 v[96:97], v216 offset:4096\n"
    "ds_read_b64_tr_b16 v[98:99], v216 offset:6144\n"
    "s_waitcnt lgkmcnt(10)\n"
    "v_mfma_f32_32x32x16_bf16 v[72:87], v[116:119], v[132:135], v[72:87]\n"
    "ds_read_b64_tr_b16 v[100:101], v217 offset:4096\n"
    "ds_read_b64_tr_b16 v[102:103], v217 offset:6144\n"
    "s_waitcnt vmcnt(12) lgkmcnt(10)\n"
    "v_mfma_f32_32x32x16_bf16 v[40:55], v[26:29], v[168:171], v[40:55]\n"
    "ds_read_b64_tr_b16 v[112:113], v216 offset:4608\n"
    "ds_read_b64_tr_b16 v[114:115], v216 offset:6656\n"
    "s_waitcnt lgkmcnt(10)\n"
    "v_mfma_f32_32x32x16_bf16 v[72:87], v[30:33], v[168:171], v[72:87]\n"
    "ds_read_b64_tr_b16 v[116:117], v217 offset:4608\n"
    "ds_read_b64_tr_b16 v[118:119], v217 offset:6656\n"
    "s_waitcnt vmcnt(11) lgkmcnt(10)\n"
    "v_mfma_f32_32x32x16_bf16 v[40:55], v[56:59], v[172:175], v[40:55]\n"
    "ds_read_b64_tr_b16 v[26:27], v216 offset:16384\n"
    "ds_read_b64_tr_b16 v[28:29], v216 offset:18432\n"
    "s_waitcnt lgkmcnt(10)\n"
    "v_mfma_f32_32x32x16_bf16 v[72:87], v[60:63], v[172:175], v[72:87]\n"
    "ds_read_b64_tr_b16 v[30:31], v217 offset:16384\n"
    "ds_read_b64_tr_b16 v[32:33], v217 offset:18432\n"
    "s_waitcnt vmcnt(10) lgkmcnt(10)\n"
    "v_mfma_f32_32x32x16_bf16 v[40:55], v[96:99], v[18:21], v[40:55]\n"
    "ds_read_b64_tr_b16 v[56:57], v216 offset:16896\n"
    "ds_read_b64_tr_b16 v[58:59], v216 offset:18944\n"
    "s_waitcnt lgkmcnt(10)\n"
    "v_mfma_f32_32x32x16_bf16 v[72:87], v[100:103], v[18:21], v[72:87]\n"
    "ds_read_b64_tr_b16 v[60:61], v217 offset:16896\n"
    "ds_read_b64_tr_b16 v[62:63], v217 offset:18944\n"
    "s_waitcnt vmcnt(8)\n"
    "v_cmp_ne_u64 vcc, s[76:77], v[222:223]\n"
    "s_nop 0\n"
    "s_cbranch_vccnz .Lslowh1%=\n"
    ".Lokh1%=:\n"
    "s_mov_b32 s93, s79\n"
    "s_add_u32 s88, s88, 0x7000\n"
    "s_add_u32 s93, s93, 1\n"
    "s_cmp_eq_u32 s93, 7\n"
    "s_cselect_b32 s93, 0, s93\n"
    "s_lshl_b32 s73, s93, 12\n"
    "s_add_u32 s73, s73, s88\n"
    "buffer_load_dwordx4 v[128:131], v2, s[52:55], s73 offen sc1\n"
    "s_waitcnt lgkmcnt(10)\n"
    "v_mfma_f32_32x32x16_bf16 v[40:55], v[112:115], v[22:25], v[40:55]\n"
    "ds_read_b64_tr_b16 v[96:97], v216 offset:20480\n"
    "ds_read_b64_tr_b16 v[98:99], v216 offset:22528\n"
    "s_waitcnt lgkmcnt(10)\n"
    "v_mfma_f32_32x32x16_bf16 v[72:87], v[116:119], v[22:25], v[72:87]\n"
    "ds_read_b64_tr_b16 v[100:101], v217 offset:20480\n"
    "ds_read_b64_tr_b16 v[102:103], v217 offset:22528\n"
    "buffer_load_dwordx4 v[132:135], v2, s[52:55], s73 offen offset:1024 sc1\n"
    "s_waitcnt vmcnt(9) lgkmcnt(10)\n"
    "v_mfma_f32_32x32x16_bf16 v[40:55], v[26:29], v[34:37], v[40:55]\n"
    "ds_read_b64_tr_b16 v[112:113], v216 offset:20992\n"
    "ds_read_b64_tr_b16 v[114:115], v216 offset:23040\n"
    "s_waitcnt lgkmcnt(10)\n"
    "v_mfma_f32_32x32x16_bf16 v[72:87], v[30:33], v[34:37], v[72:87]\n"
    "ds_read_b64_tr_b16 v[116:117], v217 offset:20992\n"
    "ds_read_b64_tr_b16 v[118:119], v217 offset:23040\n"
    "buffer_load_dwordx4 v[168:171], v2, s[52:55], s73 offen offset:2048 sc1\n"
    "s_waitcnt vmcnt(9) lgkmcnt(10)\n"
    "v_mfma_f32_32x32x16_bf16 v[40:55], v[56:59], v[64:67], v[40:55]\n"
    "ds_read_b64_tr_b16 v[26:27], v218 offset:49408\n"
    "ds_read_b64_tr_b16 v[28:29], v218 offset:51456\n"
    "s_waitcnt lgkmcnt(10)\n"
    "v_mfma_f32_32x32x16_bf16 v[72:87], v[60:63], v[64:67], v[72:87]\n"
    "ds_read_b64_tr_b16 v[30:31], v219 offset:49408\n"
    "ds_read_b64_tr_b16 v[32:33], v219 offset:51456\n"
    "buffer_load_dwordx4 v[172:175], v2, s[52:55], s73 offen offset:3072 sc1\n"
    "s_waitcnt vmcnt(9) lgkmcnt(10)\n"
    "v_mfma_f32_32x32x16_bf16 v[40:55], v[96:99], v[88:91], v[40:55]\n"
    "ds_read_b64_tr_b16 v[56:57], v218 offset:49920\n"
    "ds_read_b64_tr_b16 v[58:59], v218 offset:51968\n"
    "s_waitcnt lgkmcnt(10)\n"
    "v_mfma_f32_32x32x16_bf16 v[72:87], v[100:103], v[88:91], v[72:87]\n"
    "ds_read_b64_tr_b16 v[60:61], v219 offset:49920\n"
    "ds_read_b64_tr_b16 v[62:63], v219 offset:51968\n"
    "s_add_u32 s93, s93, 1\n"
    "s_cmp_eq_u32 s93, 7\n"
    "s_cselect_b32 s93, 0, s93\n"
    "s_lshl_b32 s73, s93, 12\n"
    "s_add_u32 s73, s73, s88\n"
    "buffer_load_dwordx4 v[18:21], v2, s[52:55], s73 offen sc1\n"
    "s_waitcnt vmcnt(9) lgkmcnt(10)\n"
    "v_mfma_f32_32x32x16_bf16 v[40:55], v[112:115], v[92:95], v[40:55]\n"
    "ds_read_b64_tr_b16 v[96:97], v218 offset:53504\n"
    "ds_read_b64_tr_b16 v[98:99], v218 offset:55552\n"
    "s_waitcnt lgkmcnt(10)\n"
    "v_mfma_f32_32x32x16_bf16 v[72:87], v[116:119], v[92:95], v[72:87]\n"
    "ds_read_b64_tr_b16 v[100:101], v219 offset:53504\n"
    "ds_read_b64_tr_b16 v[102:103], v219 offset:55552\n"
    "buffer_load_dwordx4 v[22:25], v2, s[52:55], s73 offen offset:1024 sc1\n"
    "s_waitcnt vmcnt(9) lgkmcnt(10)\n"
    "v_mfma_f32_32x32x16_bf16 v[40:55], v[26:29], v[104:107], v[40:55]\n"
    "ds_read_b64_tr_b16 v[112:113], v218 offset:54016\n"
    "ds_read_b64_tr_b16 v[114:115], v218 offset:56064\n"
    "s_waitcnt lgkmcnt(10)\n"
    "v_mfma_f32_32x32x16_bf16 v[72:87], v[30:33], v[104:107], v[72:87]\n"
    "ds_read_b64_tr_b16 v[116:117], v219 offset:54016\n"
    "ds_read_b64_tr_b16 v[118:119], v219 offset:56064\n"
    "buffer_load_dwordx4 v[34:37], v2, s[52:55], s73 offen offset:2048 sc1\n"
    "s_waitcnt vmcnt(9) lgkmcnt(10)\n"
    "v_mfma_f32_32x32x16_bf16 v[40:55], v[56:59], v[108:111], v[40:55]\n"
    "ds_read_b64_tr_b16 v[26:27], v216 offset:49152\n"
    "ds_read_b64_tr_b16 v[28:29], v216 offset:51200\n"
    "s_waitcnt vmcnt(8) lgkmcnt(8)\n"
    "v_mfma_f32_32x32x16_bf16 v[40:55], v[96:99], v[120:123], v[40:55]\n"
    "ds_read_b64_tr_b16 v[56:57], v216 offset:49664\n"
    "ds_read_b64_tr_b16 v[58:59], v216 offset:51712\n"
    "s_waitcnt vmcnt(7) lgkmcnt(6)\n"
    "v_mfma_f32_32x32x16_bf16 v[40:55], v[112:115], v[124:127], v[40:55]\n"
    "ds_read_b64_tr_b16 v[96:97], v216 offset:53248\n"
    "ds_read_b64_tr_b16 v[98:99], v216 offset:55296\n"
    "s_barrier\n"
    "v_mfma_f32_32x32x16_bf16 v[72:87], v[60:63], v[108:111], v[72:87]\n"
    "ds_read_b64_tr_b16 v[30:31], v217 offset:49152\n"
    "ds_read_b64_tr_b16 v[32:33], v217 offset:51200\n"
    "buffer_load_dwordx4 v[64:67], v2, s[52:55], s73 offen offset:3072 sc1\n"
    "v_mfma_f32_32x32x16_bf16 v[72:87], v[100:103], v[120:123], v[72:87]\n"
    "ds_read_b64_tr_b16 v[60:61], v217 offset:49664\n"
    "ds_read_b64_tr_b16 v[62:63], v217 offset:51712\n"
    "s_add_u32 s93, s93, 1\n"
    "s_cmp_eq_u32 s93, 7\n"
    "s_cselect_b32 s93, 0, s93\n"
    "s_lshl_b32 s73, s93, 12\n"
    "s_add_u32 s73, s73, s88\n"
    "buffer_load_dwordx4 v[88:91], v2, s[52:55], s73 offen sc1\n"
    "ds_write_b128 v10, v[40:43]\n"
    "ds_write_b128 v11, v[44:47]\n"
    "s_waitcnt lgkmcnt(12)\n"
    "v_mfma_f32_32x32x16_bf16 v[72:87], v[116:119], v[124:127], v[72:87]\n"
    "ds_read_b64_tr_b16 v[100:101], v217 offset:53248\n"
    "ds_read_b64_tr_b16 v[102:103], v217 offset:55296\n"
    "buffer_load_dwordx4 v[92:95], v2, s[52:55], s73 offen offset:1024 sc1\n"
    "ds_write_b128 v12, v[48:51]\n"
    "ds_write_b128 v13, v[52:55]\n"
    "s_waitcnt vmcnt(9) lgkmcnt(14)\n"
    "v_mfma_f32_32x32x16_bf16 v[136:151], v[26:29], v[128:131], v[136:151]\n"
    "ds_read_b64_tr_b16 v[112:113], v216 offset:53760\n"
    "ds_read_b64_tr_b16 v[114:115], v216 offset:55808\n"
    "s_nop 2\n"
    "ds_write_b128 v10, v[72:75] offset:128\n"
    "ds_write_b128 v11, v[76:79] offset:128\n"
    "s_waitcnt lgkmcnt(12)\n"
    "v_mfma_f32_32x32x16_bf16 v[152:167], v[30:33], v[128:131], v[152:167]\n"
    "ds_read_b64_tr_b16 v[116:117], v217 offset:53760\n"
    "ds_read_b64_tr_b16 v[118:119], v217 offset:55808\n"
    "buffer_load_dwordx4 v[104:107], v2, s[52:55], s73 offen offset:2048 sc1\n"
    "ds_write_b128 v12, v[80:83] offset:128\n"
    "ds_write_b128 v13, v[84:87] offset:128\n"
    "s_waitcnt lgkmcnt(0)\n"
    "s_barrier\n"
    "s_waitcnt vmcnt(9)\n"
    "v_mfma_f32_32x32x16_bf16 v[136:151], v[56:59], v[132:135], v[136:151]\n"
    "ds_read_b64_tr_b16 v[26:27], v218\n"
    "ds_read_b64_tr_b16 v[28:29], v218 offset:2048\n"
    "v_mfma_f32_32x32x16_bf16 v[152:167], v[60:63], v[132:135], v[152:167]\n"
    "ds_read_b64_tr_b16 v[30:31], v219\n"
    "ds_read_b64_tr_b16 v[32:33], v219 offset:2048\n"
    "buffer_load_dwordx4 v[108:111], v2, s[52:55], s73 offen offset:3072 sc1\n"
    "s_waitcnt vmcnt(9)\n"
    "v_mfma_f32_32x32x16_bf16 v[136:151], v[96:99], v[168:171], v[136:151]\n"
    "ds_read_b64_tr_b16 v[56:57], v218 offset:512\n"
    "ds_read_b64_tr_b16 v[58:59], v218 offset:2560\n"
    "v_mfma_f32_32x32x16_bf16 v[152:167], v[100:103], v[168:171], v[152:167]\n"
    "ds_read_b64_tr_b16 v[60:61], v219 offset:512\n"
    "ds_read_b64_tr_b16 v[62:63], v219 offset:2560\n"
    "s_add_u32 s93, s93, 1\n"
    "s_cmp_eq_u32 s93, 7\n"
    "s_cselect_b32 s93, 0, s93\n"
    "s_lshl_b32 s73, s93, 12\n"
    "s_add_u32 s73, s73, s88\n"
    "buffer_load_dwordx4 v[120:123], v2, s[52:55], s73 offen sc1\n"
    "s_waitcnt vmcnt(9)\n"
    "v_mfma_f32_32x32x16_bf16 v[136:151], v[112:115], v[172:175], v[136:151]\n"
    "ds_read_b64_tr_b16 v[96:97], v218 offset:4096\n"
    "ds_read_b64_tr_b16 v[98:99], v218 offset:6144\n"
    "v_mfma_f32_32x32x16_bf16 v[152:167], v[116:119], v[172:175], v[152:167]\n"
    "ds_read_b64_tr_b16 v[100:101], v219 offset:4096\n"
    "ds_read_b64_tr_b16 v[102:103], v219 offset:6144\n"
    "buffer_load_dwordx4 v[124:127], v2, s[52:55], s73 offen offset:1024 sc1\n"
    "s_waitcnt vmcnt(9) lgkmcnt(10)\n"
    "v_mfma_f32_32x32x16_bf16 v[136:151], v[26:29], v[18:21], v[136:151]\n"
    "ds_read_b64_tr_b16 v[112:113], v218 offset:4608\n"
    "ds_read_b64_tr_b16 v[114:115], v218 offset:6656\n"
    "s_waitcnt lgkmcnt(10)\n"
    "v_mfma_f32_32x32x16_bf16 v[152:167], v[30:33], v[18:21], v[152:167]\n"
    "ds_read_b64_tr_b16 v[116:117], v219 offset:4608\n"
    "ds_read_b64_tr_b16 v[118:119], v219 offset:6656\n"
    "buffer_load_dwordx4 v[128:131], v2, s[52:55], s73 offen offset:2048 sc1\n"
    "s_waitcnt vmcnt(9) lgkmcnt(10)\n"
    "v_mfma_f32_32x32x16_bf16 v[136:151], v[56:59], v[22:25], v[136:151]\n"
    "ds_read_b64_tr_b16 v[26:27], v218 offset:16384\n"
    "ds_read_b64_tr_b16 v[28:29], v218 offset:18432\n"
    "s_waitcnt lgkmcnt(10)\n"
    "v_mfma_f32_32x32x16_bf16 v[152:167], v[60:63], v[22:25], v[152:167]\n"
    "ds_read_b64_tr_b16 v[30:31], v219 offset:16384\n"
    "ds_read_b64_tr_b16 v[32:33], v219 offset:18432\n"
    "buffer_load_dwordx4 v[132:135], v2, s[52:55], s73 offen offset:3072 sc1\n"
    "s_waitcnt vmcnt(9) lgkmcnt(10)\n"
    "v_mfma_f32_32x32x16_bf16 v[136:151], v[96:99], v[34:37], v[136:151]\n"
    "ds_read_b64_tr_b16 v[56:57], v218 offset:16896\n"
    "ds_read_b64_tr_b16 v[58:59], v218 offset:18944\n"
    "s_mov_b64 exec, 1\n"
    "ds_write_b32 v71, v221\n"
    "s_mov_b64 exec, -1\n"
    "s_waitcnt lgkmcnt(11)\n"
    "v_mfma_f32_32x32x16_bf16 v[152:167], v[100:103], v[34:37], v[152:167]\n"
    "ds_read_b64_tr_b16 v[60:61], v219 offset:16896\n"
    "ds_read_b64_tr_b16 v[62:63], v219 offset:18944\n"
    "s_add_u32 s93, s93, 1\n"
    "s_cmp_eq_u32 s93, 7\n"
    "s_cselect_b32 s93, 0, s93\n"
    "s_lshl_b32 s73, s93, 12\n"
    "s_add_u32 s73, s73, s88\n"
    "buffer_load_dwordx4 v[168:171], v2, s[52:55], s73 offen sc1\n"
    "s_waitcnt vmcnt(9) lgkmcnt(11)\n"
    "v_mfma_f32_32x32x16_bf16 v[136:151], v[112:115], v[64:67], v[136:151]\n"
    "ds_read_b64_tr_b16 v[96:97], v218 offset:20480\n"
    "ds_read_b64_tr_b16 v[98:99], v218 offset:22528\n"
    "s_and_b32 s74, s69, 1\n"
    "s_waitcnt lgkmcnt(11)\n"
    "v_mfma_f32_32x32x16_bf16 v[152:167], v[116:119], v[64:67], v[152:167]\n"
    "ds_read_b64_tr_b16 v[100:101], v219 offset:20480\n"
    "ds_read_b64_tr_b16 v[102:103], v219 offset:22528\n"
    "buffer_load_dwordx4 v[172:175], v2, s[52:55], s73 offen offset:1024 sc1\n"
    "s_mul_i32 s74, s74, 0x6600\n"
    "s_waitcnt vmcnt(9) lgkmcnt(11)\n"
    "v_mfma_f32_32x32x16_bf16 v[136:151], v[26:29], v[88:91], v[136:151]\n"
    "ds_read_b64_tr_b16 v[112:113], v218 offset:20992\n"
    "ds_read_b64_tr_b16 v[114:115], v218 offset:23040\n"
    "s_add_u32 s74, s74, 0x10000\n"
    "s_waitcnt lgkmcnt(11)\n"
    "v_mfma_f32_32x32x16_bf16 v[152:167], v[30:33], v[88:91], v[152:167]\n"
    "ds_read_b64_tr_b16 v[116:117], v219 offset:20992\n"
    "ds_read_b64_tr_b16 v[118:119], v219 offset:23040\n"
    "buffer_load_dwordx4 v[18:21], v2, s[52:55], s73 offen offset:2048 sc1\n"
    "v_subrev_u32 v10, s74, v10\n"
    "s_waitcnt vmcnt(9) lgkmcnt(11)\n"
    "v_mfma_f32_32x32x16_bf16 v[136:151], v[56:59], v[92:95], v[136:151]\n"
    "ds_read_b64_tr_b16 v[26:27], v216\n"
    "ds_read_b64_tr_b16 v[28:29], v216 offset:2048\n"
    "v_subrev_u32 v11, s74, v11\n"
    "s_waitcnt lgkmcnt(10)\n"
    "v_mfma_f32_32x32x16_bf16 v[152:167], v[60:63], v[92:95], v[152:167]\n"
    "ds_read_b64_tr_b16 v[30:31], v217\n"
    "ds_read_b64_tr_b16 v[32:33], v217 offset:2048\n"
    "buffer_load_dwordx4 v[22:25], v2, s[52:55], s73 offen offset:3072 sc1\n"
    "v_subrev_u32 v12, s74, v12\n"
    "s_waitcnt vmcnt(9) lgkmcnt(10)\n"
    "v_mfma_f32_32x32x16_bf16 v[136:151], v[96:99], v[104:107], v[136:151]\n"
    "ds_read_b64_tr_b16 v[56:57], v216 offset:512\n"
    "ds_read_b64_tr_b16 v[58:59], v216 offset:2560\n"
    "v_subrev_u32 v13, s74, v13\n"
    "s_waitcnt lgkmcnt(10)\n"
    "v_mfma_f32_32x32x16_bf16 v[152:167], v[100:103], v[104:107], v[152:167]\n"
    "ds_read_b64_tr_b16 v[60:61], v217 offset:512\n"
    "ds_read_b64_tr_b16 v[62:63], v217 offset:2560\n"
    "s_add_u32 s93, s93, 1\n"
    "s_cmp_eq_u32 s93, 7\n"
    "s_cselect_b32 s93, 0, s93\n"
    "s_lshl_b32 s73, s93, 12\n"
    "s_add_u32 s73, s73, s88\n"
    "buffer_load_dwordx4 v[34:37], v2, s[52:55], s73 offen sc1\n"
    "s_waitcnt vmcnt(9) lgkmcnt(10)\n"
    "v_mfma_f32_32x32x16_bf16 v[136:151], v[112:115], v[108:111], v[136:151]\n"
    "ds_read_b64_tr_b16 v[96:97], v216 offset:4096\n"
    "ds_read_b64_tr_b16 v[98:99], v216 offset:6144\n"
    "s_waitcnt lgkmcnt(10)\n"
    "v_mfma_f32_32x32x16_bf16 v[152:167], v[116:119], v[108:111], v[152:167]\n"
    "ds_read_b64_tr_b16 v[100:101], v217 offset:4096\n"
    "ds_read_b64_tr_b16 v[102:103], v217 offset:6144\n"
    "buffer_load_dwordx4 v[64:67], v2, s[52:55], s73 offen offset:1024 sc1\n"
    "s_waitcnt vmcnt(9) lgkmcnt(10)\n"
    "v_mfma_f32_32x32x16_bf16 v[136:151], v[26:29], v[120:123], v[136:151]\n"
    "ds_read_b64_tr_b16 v[112:113], v216 offset:4608\n"
    "ds_read_b64_tr_b16 v[114:115], v216 offset:6656\n"
    "s_waitcnt lgkmcnt(10)\n"
    "v_mfma_f32_32x32x16_bf16 v[152:167], v[30:33], v[120:123], v[152:167]\n"
    "ds_read_b64_tr_b16 v[116:117], v217 offset:4608\n"
    "ds_read_b64_tr_b16 v[118:119], v217 offset:6656\n"
    "buffer_load_dwordx4 v[88:91], v2, s[52:55], s73 offen offset:2048 sc1\n"
    "s_waitcnt vmcnt(9) lgkmcnt(10)\n"
    "v_mfma_f32_32x32x16_bf16 v[136:151], v[56:59], v[124:127], v[136:151]\n"
    "ds_read_b64_tr_b16 v[26:27], v216 offset:16384\n"
    "ds_read_b64_tr_b16 v[28:29], v216 offset:18432\n"
    "s_waitcnt lgkmcnt(10)\n"
    "v_mfma_f32_32x32x16_bf16 v[152:167], v[60:63], v[124:127], v[152:167]\n"
    "ds_read_b64_tr_b16 v[30:31], v217 offset:16384\n"
    "ds_read_b64_tr_b16 v[32:33], v217 offset:18432\n"
    "buffer_load_dwordx4 v[92:95], v2, s[52:55], s73 offen offset:3072 sc1\n"
    "s_waitcnt vmcnt(9) lgkmcnt(10)\n"
    "v_mfma_f32_32x32x16_bf16 v[136:151], v[96:99], v[128:131], v[136:151]\n"
    "ds_read_b64_tr_b16 v[56:57], v216 offset:16896\n"
    "ds_read_b64_tr_b16 v[58:59], v216 offset:18944\n"
    "ds_read_b32 v3, v220\n"
    "s_waitcnt lgkmcnt(11)\n"
    "v_mfma_f32_32x32x16_bf16 v[152:167], v[100:103], v[128:131], v[152:167]\n"
    "ds_read_b64_tr_b16 v[60:61], v217 offset:16896\n"
    "ds_read_b64_tr_b16 v[62:63], v217 offset:18944\n"
    "s_waitcnt vmcnt(8) lgkmcnt(11)\n"
    "v_mfma_f32_32x32x16_bf16 v[136:151], v[112:115], v[132:135], v[136:151]\n"
    "ds_read_b64_tr_b16 v[96:97], v216 offset:20480\n"
    "ds_read_b64_tr_b16 v[98:99], v216 offset:22528\n"
    "s_waitcnt lgkmcnt(11)\n"
    "v_mfma_f32_32x32x16_bf16 v[152:167], v[116:119], v[132:135], v[152:167]\n"
    "ds_read_b64_tr_b16 v[100:101], v217 offset:20480\n"
    "ds_read_b64_tr_b16 v[102:103], v217 offset:22528\n"
    "s_waitcnt vmcnt(7) lgkmcnt(11)\n"
    "v_mfma_f32_32x32x16_bf16 v[136:151], v[26:29], v[168:171], v[136:151]\n"
    "ds_read_b64_tr_b16 v[112:113], v216 offset:20992\n"
    "ds_read_b64_tr_b16 v[114:115], v216 offset:23040\n"
    "s_waitcnt lgkmcnt(11)\n"
    "v_mfma_f32_32x32x16_bf16 v[152:167], v[30:33], v[168:171], v[152:167]\n"
    "ds_read_b64_tr_b16 v[116:117], v217 offset:20992\n"
    "ds_read_b64_tr_b16 v[118:119], v217 offset:23040\n"
    "s_waitcnt vmcnt(6) lgkmcnt(11)\n"
    "v_mfma_f32_32x32x16_bf16 v[136:151], v[56:59], v[172:175], v[136:151]\n"
    "ds_read_b64_tr_b16 v[26:27], v218 offset:49408\n"
    "ds_read_b64_tr_b16 v[28:29], v218 offset:51456\n"
    "s_waitcnt lgkmcnt(12)\n"
    "v_cmp_eq_u32 vcc, 0, v3\n"
    "s_nop 0\n"
    "s_cbranch_vccnz .Lslows2%=\n"
    ".Loks2%=:\n"
    "s_waitcnt lgkmcnt(10)\n"
    "v_mfma_f32_32x32x16_bf16 v[152:167], v[60:63], v[172:175], v[152:167]\n"
    "ds_read_b64_tr_b16 v[30:31], v219 offset:49408\n"
    "ds_read_b64_tr_b16 v[32:33], v219 offset:51456\n"
    "s_waitcnt vmcnt(5) lgkmcnt(10)\n"
    "v_mfma_f32_32x32x16_bf16 v[136:151], v[96:99], v[18:21], v[136:151]\n"
    "ds_read_b64_tr_b16 v[56:57], v218 offset:49920\n"
    "ds_read_b64_tr_b16 v[58:59], v218 offset:51968\n"
    "s_waitcnt lgkmcnt(10)\n"
    "v_mfma_f32_32x32x16_bf16 v[152:167], v[100:103], v[18:21], v[152:167]\n"
    "ds_read_b64_tr_b16 v[60:61], v219 offset:49920\n"
    "ds_read_b64_tr_b16 v[62:63], v219 offset:51968\n"
    "s_waitcnt vmcnt(4) lgkmcnt(10)\n"
    "v_mfma_f32_32x32x16_bf16 v[136:151], v[112:115], v[22:25], v[136:151]\n"
    "ds_read_b64_tr_b16 v[96:97], v218 offset:53504\n"
    "ds_read_b64_tr_b16 v[98:99], v218 offset:55552\n"
    "s_waitcnt lgkmcnt(10)\n"
    "v_mfma_f32_32x32x16_bf16 v[152:167], v[116:119], v[22:25], v[152:167]\n"
    "ds_read_b64_tr_b16 v[100:101], v219 offset:53504\n"
    "ds_read_b64_tr_b16 v[102:103], v219 offset:55552\n"
    "s_waitcnt vmcnt(3) lgkmcnt(10)\n"
    "v_mfma_f32_32x32x16_bf16 v[136:151], v[26:29], v[34:37], v[136:151]\n"
    "ds_read_b64_tr_b16 v[112:113], v218 offset:54016\n"
    "ds_read_b64_tr_b16 v[114:115], v218 offset:56064\n"
    "s_waitcnt lgkmcnt(10)\n"
    "v_mfma_f32_32x32x16_bf16 v[152:167], v[30:33], v[34:37], v[152:167]\n"
    "ds_read_b64_tr_b16 v[116:117], v219 offset:54016\n"
    "ds_read_b64_tr_b16 v[118:119], v219 offset:56064\n"
    "s_waitcnt vmcnt(2) lgkmcnt(10)\n"
    "v_mfma_f32_32x32x16_bf16 v[136:151], v[56:59], v[64:67], v[136:151]\n"
    "s_waitcnt vmcnt(1) lgkmcnt(6)\n"
    "v_mfma_f32_32x32x16_bf16 v[136:151], v[96:99], v[88:91], v[136:151]\n"
    "s_waitcnt vmcnt(0) lgkmcnt(2)\n"
    "v_mfma_f32_32x32x16_bf16 v[136:151], v[112:115], v[92:95], v[136:151]\n"
    "v_mfma_f32_32x32x16_bf16 v[152:167], v[60:63], v[64:67], v[152:167]\n"
    "v_mfma_f32_32x32x16_bf16 v[152:167], v[100:103], v[88:91], v[152:167]\n"
    "s_nop 9\n"
    "ds_write_b128 v10, v[136:139]\n"
    "ds_write_b128 v11, v[140:143]\n"
    "s_waitcnt lgkmcnt(2)\n"
    "v_mfma_f32_32x32x16_bf16 v[152:167], v[116:119], v[92:95], v[152:167]\n"
    "ds_write_b128 v12, v[144:147]\n"
    "ds_write_b128 v13, v[148:151]\n"
    "s_nop 9\n"
    "ds_write_b128 v10, v[152:155] offset:128\n"
    "ds_write_b128 v11, v[156:159] offset:128\n"
    "ds_write_b128 v12, v[160:163] offset:128\n"
    "ds_write_b128 v13, v[164:167] offset:128\n"
    "s_waitcnt lgkmcnt(0)\n"
    "s_barrier\n"
    "s_endpgm\n"
    ".Lssm%=:\n"
    "v_lshrrev_b32 v49, 4, v1\n"
    "v_and_b32 v50, 15, v1\n"
    "v_and_b32 v48, 0xff, v0\n"
    "s_lshl1_add_u32 s74, s89, 0x18000\n"
    "v_lshl_add_u32 v18, v1, 3, s74\n"
    "v_xor_b32 v19, 32, v18\n"
    "v_xor_b32 v20, 64, v18\n"
    "v_xor_b32 v21, 0x60, v18\n"
    "v_xor_b32 v22, 0x80, v18\n"
    "v_xor_b32 v23, 0xa0, v18\n"
    "v_xor_b32 v24, 0xc0, v18\n"
    "v_xor_b32 v25, 0xe0, v18\n"
    "s_waitcnt lgkmcnt(0)\n"
    "s_buffer_load_dword s92, s[32:35], s91\n"
    "buffer_load_dword v10, v16, s[8:11], s90 offen\n"
    "buffer_load_dword v11, v16, s[12:15], s90 offen\n"
    "buffer_load_dword v12, v16, s[16:19], s90 offen\n"
    "buffer_load_dword v13, v16, s[20:23], s90 offen\n"
    "buffer_load_dword v14, v16, s[24:27], s90 offen\n"
    "buffer_load_dword v15, v16, s[28:31], s90 offen\n"
    "s_buffer_load_dword s93, s[36:39], s91\n"
    "s_buffer_load_dwordx2 s[76:77], s[52:55], 0x0\n"
    "s_barrier\n"
    "v_and_b32 v3, 7, v1\n"
    "v_lshlrev_b32 v3, 1, v3\n"
    "v_xor_b32 v3, v3, v49\n"
    "v_lshlrev_b32 v3, 4, v3\n"
    "v_lshl_add_u32 v3, v50, 9, v3\n"
    "v_add_u32 v52, s74, v3\n"
    "v_xor_b32 v53, 64, v52\n"
    "v_xor_b32 v54, 0x80, v52\n"
    "v_xor_b32 v55, 0xc0, v52\n"
    "s_and_b32 s74, s89, 0x2000\n"
    "s_and_b32 s75, s69, 1\n"
    "s_lshl2_add_u32 s75, s75, s74\n"
    "s_add_u32 s75, s75, 0x22620\n"
    "v_and_b32 v4, 1, v49\n"
    "v_lshlrev_b32 v5, 7, v17\n"
    "v_lshl_add_u32 v5, v4, 2, v5\n"
    "v_lshl_add_u32 v5, v50, 3, v5\n"
    "v_lshrrev_b32 v3, 4, v5\n"
    "v_add_u32 v5, v5, v3\n"
    "v_lshl_add_u32 v51, v5, 3, s75\n"
    "v_sub_u32 v4, 2, v17\n"
    "v_lshlrev_b32 v4, v4, v50\n"
    "v_lshl_add_u32 v4, v49, 6, v4\n"
    "v_lshrrev_b32 v3, 4, v4\n"
    "v_add_u32 v4, v4, v3\n"
    "s_add_u32 s75, s75, 0x880\n"
    "v_lshl_add_u32 v56, v4, 3, s75\n"
    "v_lshl_add_u32 v57, v17, 8, v56\n"
    "v_mul_u32_u24 v4, 0x44, v49\n"
    "v_add_u32 v4, v4, v50\n"
    "s_add_u32 s75, s75, 0x880\n"
    "v_lshl_add_u32 v58, v4, 3, s75\n"
    "v_mov_b32 v60, 0\n"
    "v_mov_b32 v61, 0\n"
    "v_mov_b32 v62, 0\n"
    "v_mov_b32 v63, 0\n"
    "v_mul_u32_u24 v59, 0x1000, v17\n"
    "v_add_u32 v59, v59, v2\n"
    "v_add_u32 v59, 0x20200, v59\n"
    "v_add_u32 v218, 0x27f00, v2\n"
    "v_bfe_u32 v3, v1, 1, 1\n"
    "v_and_b32 v4, 16, v2\n"
    "v_lshl_add_u32 v3, v3, 13, v4\n"
    "v_add_u32 v3, 0x22600, v3\n"
    "v_cmp_lt_u32 vcc, 15, v1\n"
    "s_nop 1\n"
    "v_cndmask_b32 v218, v218, v3, vcc\n"
    "v_bfe_u32 v3, v1, 1, 2\n"
    "v_mul_u32_u24 v3, 0x1040, v3\n"
    "v_add_u32 v3, v3, v4\n"
    "v_add_u32 v3, 0x19000, v3\n"
    "v_cmp_lt_u32 vcc, 31, v1\n"
    "s_nop 1\n"
    "v_cndmask_b32 v218, v218, v3, vcc\n"
    "v_and_b32 v238, 0x7f, v0\n"
    "v_lshlrev_b32 v238, 4, v238\n"
    "v_sub_u32 v238, 0x7ff, v238\n"
    "v_cvt_f32_u32 v219, v238\n"
    "v_lshl_or_b32 v238, v238, 16, v238\n"
    "v_subrev_u32 v238, 0x10000, v238\n"
    "s_and_b32 s74, s89, 0x2000\n"
    "s_add_u32 s74, s74, 0x2620\n"
    "s_mul_i32 s72, s74, 0x10001\n"
    "s_mov_b32 s73, 0xbd80bd80\n"
    "v_mov_b32 v241, 0x7f007f\n"
    "v_mov_b32 v242, 0x20002\n"
    "v_mov_b32 v239, 0x80008\n"
    "v_mov_b32 v243, 0x40004\n"
    "v_mov_b32 v240, 0xffff\n"
    "s_mov_b32 s75, 0x20000\n"
    "v_add_f32 v220, 0, v219\n"
    "v_add_f32 v221, -1.0, v219\n"
    "v_cvt_pk_bf16_f32 v220, v220, v221\n"
    "v_pk_add_i16 v220, v220, s73\n"
    "v_pk_min_i16 v221, v238, v241\n"
    "v_pk_max_i16 v220, v220, v221\n"
    "v_pk_ashrrev_i16 v221, v243, v220\n"
    "v_pk_add_i16 v220, v220, v221\n"
    "v_pk_mad_u16 v220, v220, v239, s72\n"
    "v_pk_sub_i16 v238, v238, v242\n"
    "v_alignbit_b32 v221, 2, v220, 16\n"
    "v_and_or_b32 v220, v220, v240, s75\n"
    "v_add_f32 v222, -2.0, v219\n"
    "v_add_f32 v223, 0xc0400000, v219\n"
    "v_cvt_pk_bf16_f32 v222, v222, v223\n"
    "v_pk_add_i16 v222, v222, s73\n"
    "v_pk_min_i16 v223, v238, v241\n"
    "v_pk_max_i16 v222, v222, v223\n"
    "v_pk_ashrrev_i16 v223, v243, v222\n"
    "v_pk_add_i16 v222, v222, v223\n"
    "v_pk_mad_u16 v222, v222, v239, s72\n"
    "v_pk_sub_i16 v238, v238, v242\n"
    "v_alignbit_b32 v223, 2, v222, 16\n"
    "v_and_or_b32 v222, v222, v240, s75\n"
    "v_add_f32 v224, -4.0, v219\n"
    "v_add_f32 v225, 0xc0a00000, v219\n"
    "v_cvt_pk_bf16_f32 v224, v224, v225\n"
    "v_pk_add_i16 v224, v224, s73\n"
    "v_pk_min_i16 v225, v238, v241\n"
    "v_pk_max_i16 v224, v224, v225\n"
    "v_pk_ashrrev_i16 v225, v243, v224\n"
    "v_pk_add_i16 v224, v224, v225\n"
    "v_pk_mad_u16 v224, v224, v239, s72\n"
    "v_pk_sub_i16 v238, v238, v242\n"
    "v_alignbit_b32 v225, 2, v224, 16\n"
    "v_and_or_b32 v224, v224, v240, s75\n"
    "v_add_f32 v226, 0xc0c00000, v219\n"
    "v_add_f32 v227, 0xc0e00000, v219\n"
    "v_cvt_pk_bf16_f32 v226, v226, v227\n"
    "v_pk_add_i16 v226, v226, s73\n"
    "v_pk_min_i16 v227, v238, v241\n"
    "s_lshr_b32 s74, s69, 1\n"
    "s_lshl_b32 s74, s74, 4\n"
    "s_waitcnt lgkmcnt(0)\n"
    "s_lshr_b32 s92, s92, s74\n"
    "s_lshl_b32 s92, s92, 16\n"
    "v_mov_b32 v6, s92\n"
    "v_mul_f32 v3, 0x3fb8aa3b, v6\n"
    "v_mov_b32 v5, 0x3fb8aa3b\n"
    "v_fma_f32 v4, v6, v5, -v3\n"
    "v_exp_f32 v3, v3\n"
    "v_fmac_f32 v4, 0x32a57060, v6\n"
    "v_mul_f32 v4, 0x3f317218, v4\n"
    "v_fmac_f32 v3, v3, v4\n"
    "v_cvt_pk_bf16_f32 v3, v3, v3\n"
    "v_lshlrev_b32 v26, 16, v3\n"
    "v_lshlrev_b32 v27, 16, v3\n"
    "v_mul_f32 v3, 0x3fb8aa3b, v26\n"
    "v_mul_f32 v4, 0x3e22f983, v26\n"
    "s_add_u32 s76, s76, 1\n"
    "s_addc_u32 s77, s77, 0\n"
    "v_mov_b32 v8, s76\n"
    "v_mov_b32 v9, s77\n"
    "s_waitcnt vmcnt(5)\n"
    "v_lshlrev_b32 v28, 16, v10\n"
    "v_and_b32 v29, 0xffff0000, v10\n"
    "s_waitcnt vmcnt(4)\n"
    "v_lshlrev_b32 v30, 16, v11\n"
    "v_and_b32 v31, 0xffff0000, v11\n"
    "v_mul_f32 v28, v3, v28\n"
    "v_mul_f32 v29, v3, v29\n"
    "v_mul_f32 v30, v4, v30\n"
    "v_mul_f32 v31, v4, v31\n"
    "s_lshr_b32 s93, s93, s74\n"
    "s_lshl_b32 s93, s93, 16\n"
    "v_exp_f32 v32, v28\n"
    "v_fract_f32 v34, v30\n"
    "v_exp_f32 v33, v29\n"
    "v_fract_f32 v35, v31\n"
    "v_cos_f32 v96, v34\n"
    "v_sin_f32 v98, v34\n"
    "v_cos_f32 v97, v35\n"
    "v_sin_f32 v99, v35\n"
    "v_pk_mul_f32 v[96:97], v[96:97], v[32:33]\n"
    "v_pk_mul_f32 v[98:99], v[98:99], v[32:33]\n"
    "s_waitcnt vmcnt(3)\n"
    "v_lshlrev_b32 v64, 16, v12\n"
    "v_and_b32 v65, 0xffff0000, v12\n"
    "s_waitcnt vmcnt(2)\n"
    "v_lshlrev_b32 v66, 16, v13\n"
    "v_and_b32 v67, 0xffff0000, v13\n"
    "s_waitcnt vmcnt(1)\n"
    "v_lshlrev_b32 v68, 16, v14\n"
    "v_and_b32 v69, 0xffff0000, v14\n"
    "s_waitcnt vmcnt(0)\n"
    "v_lshlrev_b32 v70, 16, v15\n"
    "v_and_b32 v71, 0xffff0000, v15\n"
    "v_pk_mul_f32 v[64:65], v[64:65], v[26:27]\n"
    "v_pk_mul_f32 v[66:67], v[66:67], v[26:27]\n"
    "v_pk_mul_f32 v[32:33], v[68:69], v[64:65]\n"
    "v_pk_mul_f32 v[34:35], v[68:69], v[66:67]\n"
    "v_pk_fma_f32 v[106:107], v[70:71], v[66:67], v[32:33] neg_lo:[1,0,0] neg_hi:[1,0,0]\n"
    "v_pk_fma_f32 v[108:109], v[70:71], v[64:65], v[34:35] neg_lo:[1,0,1] neg_hi:[1,0,1]\n"
    "v_cvt_pk_bf16_f32 v38, v106, v107\n"
    "v_cvt_pk_bf16_f32 v39, v108, v109\n"
    "ds_write_b64 v18, v[38:39]\n"
    "v_pk_mul_f32 v[110:111], v[106:107], v[96:97]\n"
    "v_pk_mul_f32 v[112:113], v[108:109], v[96:97]\n"
    "v_pk_fma_f32 v[110:111], v[108:109], v[98:99], v[110:111]\n"
    "v_pk_fma_f32 v[112:113], v[106:107], v[98:99], v[112:113] neg_lo:[1,0,0] neg_hi:[1,0,0]\n"
    "v_cvt_pk_bf16_f32 v36, v110, v111\n"
    "v_cvt_pk_bf16_f32 v37, v112, v113\n"
    "ds_write_b64 v19, v[36:37] offset:512\n"
    "v_pk_mul_f32 v[114:115], v[110:111], v[96:97]\n"
    "v_pk_mul_f32 v[116:117], v[112:113], v[96:97]\n"
    "v_pk_fma_f32 v[114:115], v[112:113], v[98:99], v[114:115]\n"
    "v_pk_fma_f32 v[116:117], v[110:111], v[98:99], v[116:117] neg_lo:[1,0,0] neg_hi:[1,0,0]\n"
    "v_cvt_pk_bf16_f32 v38, v114, v115\n"
    "v_cvt_pk_bf16_f32 v39, v116, v117\n"
    "ds_write_b64 v20, v[38:39] offset:1024\n"
    "v_pk_mul_f32 v[118:119], v[114:115], v[96:97]\n"
    "v_pk_mul_f32 v[120:121], v[116:117], v[96:97]\n"
    "v_pk_fma_f32 v[118:119], v[116:117], v[98:99], v[118:119]\n"
    "v_pk_fma_f32 v[120:121], v[114:115], v[98:99], v[120:121] neg_lo:[1,0,0] neg_hi:[1,0,0]\n"
    "v_cvt_pk_bf16_f32 v36, v118, v119\n"
    "v_cvt_pk_bf16_f32 v37, v120, v121\n"
    "ds_write_b64 v21, v[36:37] offset:1536\n"
    "v_pk_mul_f32 v[122:123], v[118:119], v[96:97]\n"
    "v_pk_mul_f32 v[124:125], v[120:121], v[96:97]\n"
    "v_pk_fma_f32 v[122:123], v[120:121], v[98:99], v[122:123]\n"
    "v_pk_fma_f32 v[124:125], v[118:119], v[98:99], v[124:125] neg_lo:[1,0,0] neg_hi:[1,0,0]\n"
    "v_cvt_pk_bf16_f32 v38, v122, v123\n"
    "v_cvt_pk_bf16_f32 v39, v124, v125\n"
    "ds_write_b64 v22, v[38:39] offset:2048\n"
    "v_pk_mul_f32 v[126:127], v[122:123], v[96:97]\n"
    "v_pk_mul_f32 v[128:129], v[124:125], v[96:97]\n"
    "v_pk_fma_f32 v[126:127], v[124:125], v[98:99], v[126:127]\n"
    "v_pk_fma_f32 v[128:129], v[122:123], v[98:99], v[128:129] neg_lo:[1,0,0] neg_hi:[1,0,0]\n"
    "v_cvt_pk_bf16_f32 v36, v126, v127\n"
    "v_cvt_pk_bf16_f32 v37, v128, v129\n"
    "ds_write_b64 v23, v[36:37] offset:2560\n"
    "v_pk_mul_f32 v[130:131], v[126:127], v[96:97]\n"
    "v_pk_mul_f32 v[132:133], v[128:129], v[96:97]\n"
    "v_pk_fma_f32 v[130:131], v[128:129], v[98:99], v[130:131]\n"
    "v_pk_fma_f32 v[132:133], v[126:127], v[98:99], v[132:133] neg_lo:[1,0,0] neg_hi:[1,0,0]\n"
    "v_cvt_pk_bf16_f32 v38, v130, v131\n"
    "v_cvt_pk_bf16_f32 v39, v132, v133\n"
    "ds_write_b64 v24, v[38:39] offset:3072\n"
    "v_pk_mul_f32 v[134:135], v[130:131], v[96:97]\n"
    "v_pk_mul_f32 v[136:137], v[132:133], v[96:97]\n"
    "v_pk_fma_f32 v[134:135], v[132:133], v[98:99], v[134:135]\n"
    "v_pk_fma_f32 v[136:137], v[130:131], v[98:99], v[136:137] neg_lo:[1,0,0] neg_hi:[1,0,0]\n"
    "v_cvt_pk_bf16_f32 v36, v134, v135\n"
    "v_cvt_pk_bf16_f32 v37, v136, v137\n"
    "ds_write_b64 v25, v[36:37] offset:3584\n"
    "v_mul_f32 v32, 0x43000000, v28\n"
    "v_mul_f32 v34, 0x43000000, v30\n"
    "v_exp_f32 v32, v32\n"
    "v_fract_f32 v34, v34\n"
    "v_mul_f32 v33, 0x43000000, v29\n"
    "v_mul_f32 v35, 0x43000000, v31\n"
    "v_exp_f32 v33, v33\n"
    "v_fract_f32 v35, v35\n"
    "v_cos_f32 v100, v34\n"
    "v_sin_f32 v102, v34\n"
    "v_cos_f32 v101, v35\n"
    "v_sin_f32 v103, v35\n"
    "v_pk_mul_f32 v[100:101], v[100:101], v[32:33]\n"
    "v_pk_mul_f32 v[102:103], v[102:103], v[32:33]\n"
    "v_sub_f32 v104, 0, v102\n"
    "v_sub_f32 v105, 0, v103\n"
    "v_pk_mul_f32 v[138:139], v[106:107], v[100:101]\n"
    "v_pk_mul_f32 v[140:141], v[108:109], v[100:101]\n"
    "v_pk_fma_f32 v[138:139], v[108:109], v[102:103], v[138:139]\n"
    "v_pk_fma_f32 v[140:141], v[106:107], v[102:103], v[140:141] neg_lo:[1,0,0] neg_hi:[1,0,0]\n"
    "v_cvt_pk_bf16_f32 v38, v138, v139\n"
    "v_cvt_pk_bf16_f32 v39, v140, v141\n"
    "ds_write_b64 v18, v[38:39] offset:4096\n"
    "v_pk_mul_f32 v[106:107], v[110:111], v[100:101]\n"
    "v_pk_mul_f32 v[108:109], v[112:113], v[100:101]\n"
    "v_pk_fma_f32 v[106:107], v[112:113], v[102:103], v[106:107]\n"
    "v_pk_fma_f32 v[108:109], v[110:111], v[102:103], v[108:109] neg_lo:[1,0,0] neg_hi:[1,0,0]\n"
    "v_cvt_pk_bf16_f32 v36, v106, v107\n"
    "v_cvt_pk_bf16_f32 v37, v108, v109\n"
    "ds_write_b64 v19, v[36:37] offset:4608\n"
    "v_pk_mul_f32 v[110:111], v[114:115], v[100:101]\n"
    "v_pk_mul_f32 v[112:113], v[116:117], v[100:101]\n"
    "v_pk_fma_f32 v[110:111], v[116:117], v[102:103], v[110:111]\n"
    "v_pk_fma_f32 v[112:113], v[114:115], v[102:103], v[112:113] neg_lo:[1,0,0] neg_hi:[1,0,0]\n"
    "v_cvt_pk_bf16_f32 v38, v110, v111\n"
    "v_cvt_pk_bf16_f32 v39, v112, v113\n"
    "ds_write_b64 v20, v[38:39] offset:5120\n"
    "v_pk_mul_f32 v[114:115], v[118:119], v[100:101]\n"
    "v_pk_mul_f32 v[116:117], v[120:121], v[100:101]\n"
    "v_pk_fma_f32 v[114:115], v[120:121], v[102:103], v[114:115]\n"
    "v_pk_fma_f32 v[116:117], v[118:119], v[102:103], v[116:117] neg_lo:[1,0,0] neg_hi:[1,0,0]\n"
    "v_cvt_pk_bf16_f32 v36, v114, v115\n"
    "v_cvt_pk_bf16_f32 v37, v116, v117\n"
    "ds_write_b64 v21, v[36:37] offset:5632\n"
    "v_pk_mul_f32 v[118:119], v[122:123], v[100:101]\n"
    "v_pk_mul_f32 v[120:121], v[124:125], v[100:101]\n"
    "v_pk_fma_f32 v[118:119], v[124:125], v[102:103], v[118:119]\n"
    "v_pk_fma_f32 v[120:121], v[122:123], v[102:103], v[120:121] neg_lo:[1,0,0] neg_hi:[1,0,0]\n"
    "v_cvt_pk_bf16_f32 v38, v118, v119\n"
    "v_cvt_pk_bf16_f32 v39, v120, v121\n"
    "ds_write_b64 v22, v[38:39] offset:6144\n"
    "v_pk_mul_f32 v[122:123], v[126:127], v[100:101]\n"
    "v_pk_mul_f32 v[124:125], v[128:129], v[100:101]\n"
    "v_pk_fma_f32 v[122:123], v[128:129], v[102:103], v[122:123]\n"
    "v_pk_fma_f32 v[124:125], v[126:127], v[102:103], v[124:125] neg_lo:[1,0,0] neg_hi:[1,0,0]\n"
    "v_cvt_pk_bf16_f32 v36, v122, v123\n"
    "v_cvt_pk_bf16_f32 v37, v124, v125\n"
    "ds_write_b64 v23, v[36:37] offset:6656\n"
    "v_pk_mul_f32 v[126:127], v[130:131], v[100:101]\n"
    "v_pk_mul_f32 v[128:129], v[132:133], v[100:101]\n"
    "v_pk_fma_f32 v[126:127], v[132:133], v[102:103], v[126:127]\n"
    "v_pk_fma_f32 v[128:129], v[130:131], v[102:103], v[128:129] neg_lo:[1,0,0] neg_hi:[1,0,0]\n"
    "v_cvt_pk_bf16_f32 v38, v126, v127\n"
    "v_cvt_pk_bf16_f32 v39, v128, v129\n"
    "ds_write_b64 v24, v[38:39] offset:7168\n"
    "v_pk_mul_f32 v[130:131], v[134:135], v[100:101]\n"
    "v_pk_mul_f32 v[132:133], v[136:137], v[100:101]\n"
    "v_pk_fma_f32 v[130:131], v[136:137], v[102:103], v[130:131]\n"
    "v_pk_fma_f32 v[132:133], v[134:135], v[102:103], v[132:133] neg_lo:[1,0,0] neg_hi:[1,0,0]\n"
    "v_cvt_pk_bf16_f32 v36, v130, v131\n"
    "v_cvt_pk_bf16_f32 v37, v132, v133\n"
    "ds_write_b64 v25, v[36:37] offset:7680\n"
    "ds_read_b128 v[186:189], v52\n"
    "ds_read_b128 v[190:193], v53\n"
    "ds_read_b128 v[194:197], v54\n"
    "ds_read_b128 v[198:201], v55\n"
    "ds_read_b128 v[202:205], v52 offset:256\n"
    "ds_read_b128 v[206:209], v53 offset:256\n"
    "ds_read_b128 v[210:213], v54 offset:256\n"
    "ds_read_b128 v[214:217], v55 offset:256\n"
    "s_waitcnt lgkmcnt(0)\n"
    "s_barrier\n"
    "s_setprio 0\n"
    "ds_read_b128 v[154:157], v52 offset:32768\n"
    "ds_read_b128 v[158:161], v53 offset:32768\n"
    "ds_read_b128 v[162:165], v54 offset:32768\n"
    "ds_read_b128 v[166:169], v55 offset:32768\n"
    "ds_read_b128 v[170:173], v52 offset:33024\n"
    "ds_read_b128 v[174:177], v53 offset:33024\n"
    "ds_read_b128 v[178:181], v54 offset:33024\n"
    "ds_read_b128 v[182:185], v55 offset:33024\n"
    "s_andn2_b32 s90, 1, s69\n"
    "s_mov_b32 s91, 0\n"
    "v_mov_b32 v3, s93\n"
    "v_cndmask_b32 v142, 0, v3, s[90:91]\n"
    "v_mov_b32 v143, 0\n"
    "v_mov_b32 v144, 0\n"
    "v_mov_b32 v145, 0\n"
    "v_mul_f32 v134, v138, v100\n"
    "v_mul_f32 v135, v139, v101\n"
    "v_mul_f32 v136, v140, v100\n"
    "v_mul_f32 v137, v141, v101\n"
    "v_fmac_f32 v134, v140, v102\n"
    "v_fmac_f32 v135, v141, v103\n"
    "v_fmac_f32 v136, v138, v104\n"
    "v_fmac_f32 v137, v139, v105\n"
    "v_cvt_pk_bf16_f32 v64, v134, v135\n"
    "v_cvt_pk_bf16_f32 v65, v136, v137\n"
    "v_mul_f32 v138, v110, v100\n"
    "v_mul_f32 v139, v111, v101\n"
    "v_mul_f32 v140, v112, v100\n"
    "v_mul_f32 v141, v113, v101\n"
    "v_fmac_f32 v138, v112, v102\n"
    "v_fmac_f32 v139, v113, v103\n"
    "v_fmac_f32 v140, v110, v104\n"
    "v_fmac_f32 v141, v111, v105\n"
    "v_cvt_pk_bf16_f32 v66, v138, v139\n"
    "v_cvt_pk_bf16_f32 v67, v140, v141\n"
    "v_mul_f32 v110, v118, v100\n"
    "v_mul_f32 v111, v119, v101\n"
    "v_mul_f32 v112, v120, v100\n"
    "v_mul_f32 v113, v121, v101\n"
    "v_fmac_f32 v110, v120, v102\n"
    "v_fmac_f32 v111, v121, v103\n"
    "v_fmac_f32 v112, v118, v104\n"
    "s_waitcnt lgkmcnt(0)\n"
    "s_barrier\n"
    "v_fmac_f32 v113, v119, v105\n"
    "v_cvt_pk_bf16_f32 v68, v110, v111\n"
    "v_cvt_pk_bf16_f32 v69, v112, v113\n"
    "v_mul_f32 v118, v126, v100\n"
    "v_mul_f32 v119, v127, v101\n"
    "v_mul_f32 v120, v128, v100\n"
    "v_mul_f32 v121, v129, v101\n"
    "v_fmac_f32 v118, v128, v102\n"
    "v_fmac_f32 v119, v129, v103\n"
    "v_fmac_f32 v120, v126, v104\n"
    "v_fmac_f32 v121, v127, v105\n"
    "v_cvt_pk_bf16_f32 v70, v118, v119\n"
    "v_cvt_pk_bf16_f32 v71, v120, v121\n"
    "v_mul_f32 v126, v134, v100\n"
    "s_setprio 3\n"
    "v_mfma_f32_16x16x32_bf16 v[142:145], v[186:189], v[154:157], v[142:145]\n"
    "v_mfma_f32_16x16x32_bf16 v[142:145], v[190:193], v[158:161], v[142:145]\n"
    "s_setprio 0\n"
    "v_mul_f32 v127, v135, v101\n"
    "v_mul_f32 v128, v136, v100\n"
    "v_mul_f32 v129, v137, v101\n"
    "v_fmac_f32 v126, v136, v102\n"
    "v_fmac_f32 v127, v137, v103\n"
    "v_fmac_f32 v128, v134, v104\n"
    "v_fmac_f32 v129, v135, v105\n"
    "v_cvt_pk_bf16_f32 v72, v126, v127\n"
    "v_cvt_pk_bf16_f32 v73, v128, v129\n"
    "v_mul_f32 v134, v138, v100\n"
    "v_mul_f32 v135, v139, v101\n"
    "v_mul_f32 v136, v140, v100\n"
    "v_mul_f32 v137, v141, v101\n"
    "v_fmac_f32 v134, v140, v102\n"
    "v_fmac_f32 v135, v141, v103\n"
    "v_fmac_f32 v136, v138, v104\n"
    "v_fmac_f32 v137, v139, v105\n"
    "v_cvt_pk_bf16_f32 v74, v134, v135\n"
    "v_cvt_pk_bf16_f32 v75, v136, v137\n"
    "v_mul_f32 v138, v110, v100\n"
    "v_mul_f32 v139, v111, v101\n"
    "v_mul_f32 v140, v112, v100\n"
    "v_mul_f32 v141, v113, v101\n"
    "v_fmac_f32 v138, v112, v102\n"
    "v_fmac_f32 v139, v113, v103\n"
    "v_fmac_f32 v140, v110, v104\n"
    "v_fmac_f32 v141, v111, v105\n"
    "v_cvt_pk_bf16_f32 v76, v138, v139\n"
    "v_cvt_pk_bf16_f32 v77, v140, v141\n"
    "v_mul_f32 v110, v118, v100\n"
    "v_mul_f32 v111, v119, v101\n"
    "v_mul_f32 v112, v120, v100\n"
    "v_mul_f32 v113, v121, v101\n"
    "v_fmac_f32 v110, v120, v102\n"
    "v_fmac_f32 v111, v121, v103\n"
    "v_fmac_f32 v112, v118, v104\n"
    "v_fmac_f32 v113, v119, v105\n"
    "v_cvt_pk_bf16_f32 v78, v110, v111\n"
    "v_cvt_pk_bf16_f32 v79, v112, v113\n"
    "v_mul_f32 v118, v126, v100\n"
    "v_mul_f32 v119, v127, v101\n"
    "v_mul_f32 v120, v128, v100\n"
    "v_mul_f32 v121, v129, v101\n"
    "v_fmac_f32 v118, v128, v102\n"
    "v_fmac_f32 v119, v129, v103\n"
    "v_fmac_f32 v120, v126, v104\n"
    "v_fmac_f32 v121, v127, v105\n"
    "v_cvt_pk_bf16_f32 v80, v118, v119\n"
    "v_cvt_pk_bf16_f32 v81, v120, v121\n"
    "v_mul_f32 v126, v138, v100\n"
    "v_mul_f32 v127, v139, v101\n"
    "v_mul_f32 v128, v140, v100\n"
    "s_setprio 3\n"
    "v_mfma_f32_16x16x32_bf16 v[142:145], v[194:197], v[162:165], v[142:145]\n"
    "v_mfma_f32_16x16x32_bf16 v[142:145], v[198:201], v[166:169], v[142:145]\n"
    "s_setprio 0\n"
    "v_mul_f32 v129, v141, v101\n"
    "v_fmac_f32 v126, v140, v102\n"
    "s_barrier\n"
    "v_fmac_f32 v127, v141, v103\n"
    "v_fmac_f32 v128, v138, v104\n"
    "v_fmac_f32 v129, v139, v105\n"
    "v_cvt_pk_bf16_f32 v82, v126, v127\n"
    "v_cvt_pk_bf16_f32 v83, v128, v129\n"
    "v_mul_f32 v138, v118, v100\n"
    "v_mul_f32 v139, v119, v101\n"
    "v_mul_f32 v140, v120, v100\n"
    "v_mul_f32 v141, v121, v101\n"
    "v_fmac_f32 v138, v120, v102\n"
    "v_fmac_f32 v139, v121, v103\n"
    "v_fmac_f32 v140, v118, v104\n"
    "v_fmac_f32 v141, v119, v105\n"
    "v_cvt_pk_bf16_f32 v84, v138, v139\n"
    "v_cvt_pk_bf16_f32 v85, v140, v141\n"
    "v_mul_f32 v118, v126, v100\n"
    "v_mul_f32 v119, v127, v101\n"
    "v_mul_f32 v120, v128, v100\n"
    "v_mul_f32 v121, v129, v101\n"
    "v_fmac_f32 v118, v128, v102\n"
    "v_fmac_f32 v119, v129, v103\n"
    "v_fmac_f32 v120, v126, v104\n"
    "v_fmac_f32 v121, v127, v105\n"
    "v_cvt_pk_bf16_f32 v86, v118, v119\n"
    "v_cvt_pk_bf16_f32 v87, v120, v121\n"
    "v_mul_f32 v126, v138, v100\n"
    "v_mul_f32 v127, v139, v101\n"
    "v_mul_f32 v128, v140, v100\n"
    "v_mul_f32 v129, v141, v101\n"
    "v_fmac_f32 v126, v140, v102\n"
    "v_fmac_f32 v127, v141, v103\n"
    "v_fmac_f32 v128, v138, v104\n"
    "v_fmac_f32 v129, v139, v105\n"
    "v_cvt_pk_bf16_f32 v88, v126, v127\n"
    "v_cvt_pk_bf16_f32 v89, v128, v129\n"
    "v_mul_f32 v138, v118, v100\n"
    "v_mul_f32 v139, v119, v101\n"
    "v_mul_f32 v140, v120, v100\n"
    "v_mul_f32 v141, v121, v101\n"
    "v_fmac_f32 v138, v120, v102\n"
    "v_fmac_f32 v139, v121, v103\n"
    "v_fmac_f32 v140, v118, v104\n"
    "v_fmac_f32 v141, v119, v105\n"
    "v_cvt_pk_bf16_f32 v90, v138, v139\n"
    "v_cvt_pk_bf16_f32 v91, v140, v141\n"
    "v_mul_f32 v118, v126, v100\n"
    "v_mul_f32 v119, v127, v101\n"
    "v_mul_f32 v120, v128, v100\n"
    "v_mul_f32 v121, v129, v101\n"
    "v_fmac_f32 v118, v128, v102\n"
    "v_fmac_f32 v119, v129, v103\n"
    "s_setprio 3\n"
    "v_mfma_f32_16x16x32_bf16 v[142:145], v[202:205], v[170:173], v[142:145]\n"
    "v_mfma_f32_16x16x32_bf16 v[142:145], v[206:209], v[174:177], v[142:145]\n"
    "s_setprio 0\n"
    "v_fmac_f32 v120, v126, v104\n"
    "v_fmac_f32 v121, v127, v105\n"
    "v_cvt_pk_bf16_f32 v92, v118, v119\n"
    "v_cvt_pk_bf16_f32 v93, v120, v121\n"
    "v_mul_f32 v126, v138, v100\n"
    "v_mul_f32 v127, v139, v101\n"
    "v_mul_f32 v128, v140, v100\n"
    "v_mul_f32 v129, v141, v101\n"
    "v_fmac_f32 v126, v140, v102\n"
    "v_fmac_f32 v127, v141, v103\n"
    "v_fmac_f32 v128, v138, v104\n"
    "v_fmac_f32 v129, v139, v105\n"
    "v_cvt_pk_bf16_f32 v94, v126, v127\n"
    "v_cvt_pk_bf16_f32 v95, v128, v129\n"
    "v_mul_f32 v138, v118, v100\n"
    "v_mul_f32 v139, v119, v101\n"
    "v_mul_f32 v140, v120, v100\n"
    "v_mul_f32 v141, v121, v101\n"
    "v_fmac_f32 v138, v120, v102\n"
    "v_fmac_f32 v139, v121, v103\n"
    "v_fmac_f32 v140, v118, v104\n"
    "v_fmac_f32 v141, v119, v105\n"
    "v_cvt_pk_bf16_f32 v106, v138, v139\n"
    "s_barrier\n"
    "v_cvt_pk_bf16_f32 v107, v140, v141\n"
    "v_mul_f32 v118, v138, v100\n"
    "v_mul_f32 v119, v139, v101\n"
    "v_mul_f32 v120, v140, v100\n"
    "v_mul_f32 v121, v141, v101\n"
    "v_fmac_f32 v118, v140, v102\n"
    "v_fmac_f32 v119, v141, v103\n"
    "v_fmac_f32 v120, v138, v104\n"
    "v_fmac_f32 v121, v139, v105\n"
    "v_cvt_pk_bf16_f32 v108, v118, v119\n"
    "v_cvt_pk_bf16_f32 v109, v120, v121\n"
    "v_mul_f32 v138, v118, v100\n"
    "v_mul_f32 v139, v119, v101\n"
    "v_mul_f32 v140, v120, v100\n"
    "v_mul_f32 v141, v121, v101\n"
    "v_fmac_f32 v138, v120, v102\n"
    "v_fmac_f32 v139, v121, v103\n"
    "v_fmac_f32 v140, v118, v104\n"
    "v_fmac_f32 v141, v119, v105\n"
    "v_cvt_pk_bf16_f32 v134, v138, v139\n"
    "v_cvt_pk_bf16_f32 v135, v140, v141\n"
    "v_mul_f32 v118, v138, v100\n"
    "v_mul_f32 v119, v139, v101\n"
    "v_mul_f32 v120, v140, v100\n"
    "v_mul_f32 v121, v141, v101\n"
    "v_fmac_f32 v118, v140, v102\n"
    "v_fmac_f32 v119, v141, v103\n"
    "v_fmac_f32 v120, v138, v104\n"
    "v_fmac_f32 v121, v139, v105\n"
    "s_setprio 3\n"
    "v_mfma_f32_16x16x32_bf16 v[142:145], v[210:213], v[178:181], v[142:145]\n"
    "v_mfma_f32_16x16x32_bf16 v[142:145], v[214:217], v[182:185], v[142:145]\n"
    "s_setprio 0\n"
    "v_cvt_pk_bf16_f32 v136, v118, v119\n"
    "v_cvt_pk_bf16_f32 v137, v120, v121\n"
    "v_mul_f32 v138, v118, v100\n"
    "v_mul_f32 v139, v119, v101\n"
    "v_mul_f32 v140, v120, v100\n"
    "v_mul_f32 v141, v121, v101\n"
    "v_fmac_f32 v138, v120, v102\n"
    "v_fmac_f32 v139, v121, v103\n"
    "v_fmac_f32 v140, v118, v104\n"
    "v_fmac_f32 v141, v119, v105\n"
    "v_cvt_pk_bf16_f32 v114, v138, v139\n"
    "v_cvt_pk_bf16_f32 v115, v140, v141\n"
    "v_mul_f32 v118, v138, v100\n"
    "v_mul_f32 v119, v139, v101\n"
    "v_mul_f32 v120, v140, v100\n"
    "v_mul_f32 v121, v141, v101\n"
    "v_fmac_f32 v118, v140, v102\n"
    "v_fmac_f32 v119, v141, v103\n"
    "v_fmac_f32 v120, v138, v104\n"
    "v_fmac_f32 v121, v139, v105\n"
    "v_cvt_pk_bf16_f32 v116, v118, v119\n"
    "v_cvt_pk_bf16_f32 v117, v120, v121\n"
    "v_mul_f32 v138, v118, v100\n"
    "v_mul_f32 v139, v119, v101\n"
    "v_mul_f32 v140, v120, v100\n"
    "v_mul_f32 v141, v121, v101\n"
    "v_fmac_f32 v138, v120, v102\n"
    "v_fmac_f32 v139, v121, v103\n"
    "v_fmac_f32 v140, v118, v104\n"
    "v_fmac_f32 v141, v119, v105\n"
    "v_cvt_pk_bf16_f32 v126, v138, v139\n"
    "v_cvt_pk_bf16_f32 v127, v140, v141\n"
    "v_mul_f32 v118, v138, v100\n"
    "v_mul_f32 v119, v139, v101\n"
    "v_mul_f32 v120, v140, v100\n"
    "v_mul_f32 v121, v141, v101\n"
    "v_fmac_f32 v118, v140, v102\n"
    "v_fmac_f32 v119, v141, v103\n"
    "v_fmac_f32 v120, v138, v104\n"
    "v_fmac_f32 v121, v139, v105\n"
    "v_cvt_pk_bf16_f32 v128, v118, v119\n"
    "v_cvt_pk_bf16_f32 v129, v120, v121\n"
    "v_mul_f32 v138, v118, v100\n"
    "v_mul_f32 v139, v119, v101\n"
    "v_mul_f32 v140, v120, v100\n"
    "s_barrier\n"
    "v_mul_f32 v141, v121, v101\n"
    "v_fmac_f32 v138, v120, v102\n"
    "v_fmac_f32 v139, v121, v103\n"
    "v_fmac_f32 v140, v118, v104\n"
    "v_fmac_f32 v141, v119, v105\n"
    "v_cvt_pk_bf16_f32 v122, v138, v139\n"
    "v_cvt_pk_bf16_f32 v123, v140, v141\n"
    "v_pk_max_i16 v226, v226, v227\n"
    "v_pk_ashrrev_i16 v227, v243, v226\n"
    "v_pk_add_i16 v226, v226, v227\n"
    "v_pk_mad_u16 v226, v226, v239, s72\n"
    "v_pk_sub_i16 v238, v238, v242\n"
    "v_alignbit_b32 v227, 2, v226, 16\n"
    "v_and_or_b32 v226, v226, v240, s75\n"
    "v_add_f32 v228, 0xc1000000, v219\n"
    "v_add_f32 v229, 0xc1100000, v219\n"
    "v_cvt_pk_bf16_f32 v228, v228, v229\n"
    "v_pk_add_i16 v228, v228, s73\n"
    "v_pk_min_i16 v229, v238, v241\n"
    "v_pk_max_i16 v228, v228, v229\n"
    "v_pk_ashrrev_i16 v229, v243, v228\n"
    "v_pk_add_i16 v228, v228, v229\n"
    "v_pk_mad_u16 v228, v228, v239, s72\n"
    "v_pk_sub_i16 v238, v238, v242\n"
    "v_alignbit_b32 v229, 2, v228, 16\n"
    "v_and_or_b32 v228, v228, v240, s75\n"
    "v_add_f32 v230, 0xc1200000, v219\n"
    "v_add_f32 v231, 0xc1300000, v219\n"
    "v_cvt_pk_bf16_f32 v230, v230, v231\n"
    "v_pk_add_i16 v230, v230, s73\n"
    "v_pk_min_i16 v231, v238, v241\n"
    "v_pk_max_i16 v230, v230, v231\n"
    "v_pk_ashrrev_i16 v231, v243, v230\n"
    "v_pk_add_i16 v230, v230, v231\n"
    "v_pk_mad_u16 v230, v230, v239, s72\n"
    "v_pk_sub_i16 v238, v238, v242\n"
    "v_alignbit_b32 v231, 2, v230, 16\n"
    "v_and_or_b32 v230, v230, v240, s75\n"
    "v_add_f32 v232, 0xc1400000, v219\n"
    "v_add_f32 v233, 0xc1500000, v219\n"
    "v_cvt_pk_bf16_f32 v232, v232, v233\n"
    "v_pk_add_i16 v232, v232, s73\n"
    "v_pk_min_i16 v233, v238, v241\n"
    "v_pk_max_i16 v232, v232, v233\n"
    "v_pk_ashrrev_i16 v233, v243, v232\n"
    "v_pk_add_i16 v232, v232, v233\n"
    "v_pk_mad_u16 v232, v232, v239, s72\n"
    "v_pk_sub_i16 v238, v238, v242\n"
    "v_alignbit_b32 v233, 2, v232, 16\n"
    "v_and_or_b32 v232, v232, v240, s75\n"
    "v_add_f32 v234, 0xc1600000, v219\n"
    "v_add_f32 v235, 0xc1700000, v219\n"
    "v_cvt_pk_bf16_f32 v234, v234, v235\n"
    "v_pk_add_i16 v234, v234, s73\n"
    "v_pk_min_i16 v235, v238, v241\n"
    "v_pk_max_i16 v234, v234, v235\n"
    "v_pk_ashrrev_i16 v235, v243, v234\n"
    "v_pk_add_i16 v234, v234, v235\n"
    "v_pk_mad_u16 v234, v234, v239, s72\n"
    "v_pk_sub_i16 v238, v238, v242\n"
    "v_alignbit_b32 v235, 2, v234, 16\n"
    "v_and_or_b32 v234, v234, v240, s75\n"
    "v_add_f32 v236, 0xc1800000, v219\n"
    "v_add_f32 v237, 0xc1880000, v219\n"
    "v_max_f32 v236, 0, v236\n"
    "v_max_f32 v237, 0, v237\n"
    "v_cvt_pk_bf16_f32 v236, v236, v237\n"
    "v_pk_add_i16 v236, v236, s73\n"
    "v_pk_min_i16 v237, v238, v241\n"
    "v_pk_max_i16 v236, v236, v237\n"
    "v_pk_ashrrev_i16 v237, v243, v236\n"
    "v_pk_add_i16 v236, v236, v237\n"
    "v_pk_mad_u16 v236, v236, v239, s72\n"
    "v_pk_sub_i16 v238, v238, v242\n"
    "v_alignbit_b32 v237, 2, v236, 16\n"
    "v_and_or_b32 v236, v236, v240, s75\n"
    "v_lshrrev_b32 v32, 3, v48\n"
    "v_and_b32 v16, 7, v1\n"
    "s_lshl1_add_u32 s93, s80, s81\n"
    "v_mul_u32_u24 v33, 0x700, v32\n"
    "s_barrier\n"
    "v_lshl_add_u32 v33, v16, 4, v33\n"
    "s_mul_i32 s74, s93, 0xe000\n"
    "s_lshl_b32 s75, s79, 7\n"
    "s_add_u32 s71, s74, s75\n"
    "v_lshrrev_b32 v5, 3, v1\n"
    "v_and_b32 v4, 0x70, v2\n"
    "v_lshl_add_u32 v14, v5, 8, v4\n"
    "v_add_u32 v15, 0x400, v14\n"
    "s_mul_i32 s74, s82, 0x2000\n"
    "s_lshr_b32 s75, s69, 1\n"
    "s_mul_i32 s88, s75, 0x1200\n"
    "s_add_u32 s88, s88, 0x20400\n"
    "s_lshl_b32 s75, s75, 7\n"
    "s_add_u32 s74, s74, s75\n"
    "s_and_b32 s75, s89, 0x1000\n"
    "s_add_u32 s74, s74, s75\n"
    "s_add_u32 s92, s74, 0x90000\n"
    "s_lshr_b32 s75, s75, 1\n"
    "s_add_u32 s88, s88, s75\n"
    "v_and_b32 v13, 31, v1\n"
    "v_lshlrev_b32 v13, 7, v13\n"
    "v_add_u32 v13, 0x1000, v13\n"
    "s_mov_b32 s70, 0\n"
    "v_and_b32 v12, 3, v1\n"
    "v_lshlrev_b32 v12, 2, v12\n"
    "v_add_u32 v12, 0x27f00, v12\n"
    "v_and_b32 v5, 0x7f, v0\n"
    "v_lshlrev_b32 v5, 5, v5\n"
    "s_lshr_b32 s74, s69, 1\n"
    "s_mul_i32 s74, s74, 0x2080\n"
    "s_add_u32 s74, s74, 0x18000\n"
    "v_add_u32 v10, s74, v5\n"
    "v_not_b32 v3, v50\n"
    "v_and_b32 v3, 1, v3\n"
    "v_mul_u32_u24 v3, 0x1040, v3\n"
    "v_and_b32 v4, 14, v50\n"
    "v_lshl_add_u32 v4, v17, 4, v4\n"
    "v_lshlrev_b32 v4, 1, v4\n"
    "v_sub_u32 v3, v3, v4\n"
    "v_and_b32 v4, 1, v49\n"
    "v_lshl_add_u32 v3, v4, 4, v3\n"
    "s_and_b32 s75, s69, 1\n"
    "s_lshl_b32 s75, s75, 8\n"
    "s_sub_u32 s74, s74, s75\n"
    "s_add_u32 s74, s74, 0x13c\n"
    "v_add_u32 v26, s74, v3\n"
    "v_add_u32 v27, 0x400, v26\n"
    "v_add_u32 v28, 0x800, v26\n"
    "v_add_u32 v29, 0xc00, v26\n"
    "v_sub_u32 v3, v50, v17\n"
    "v_lshlrev_b32 v3, 5, v3\n"
    "v_lshl_add_u32 v3, v4, 4, v3\n"
    "s_lshr_b32 s74, s69, 1\n"
    "s_mul_i32 s74, s74, 0x1200\n"
    "s_add_u32 s74, s74, 0x20400\n"
    "s_sub_u32 s74, s74, s75\n"
    "s_sub_u32 s74, s74, 0xc0\n"
    "v_add_u32 v11, s74, v3\n"
    "s_lshl_b32 s74, s69, 2\n"
    "s_add_u32 s74, s74, 0x27f00\n"
    "v_mov_b32 v96, s74\n"
    "s_xor_b32 s74, s74, 4\n"
    "v_mov_b32 v97, s74\n"
    "v_mov_b32 v98, 1\n"
    "s_barrier\n"
    "buffer_load_dwordx2 v[248:249], v13, s[52:55], 0 offen sc1\n"
    "buffer_load_dwordx4 v[40:43], v33, s[40:43], s71 offen\n"
    "buffer_load_dwordx4 v[44:47], v33, s[40:43], s71 offen offset:896\n"
    "s_lshl_b32 s84, s79, 7\n"
    "v_lshlrev_b32 v6, 4, v16\n"
    "buffer_load_dwordx4 v[244:247], v6, s[64:67], s84 offen\n"
    "s_mov_b32 exec_lo, 0xfffff\n"
    "s_mov_b32 exec_hi, 0\n"
    "ds_write_b128 v218, v[60:63]\n"
    "s_mov_b64 exec, -1\n"
    "ds_write_b64 v18, v[64:65]\n"
    "ds_write_b64 v19, v[66:67] offset:512\n"
    "ds_write_b64 v20, v[68:69] offset:1024\n"
    "ds_write_b64 v21, v[70:71] offset:1536\n"
    "ds_write_b64 v22, v[72:73] offset:2048\n"
    "s_waitcnt lgkmcnt(5)\n"
    "s_barrier\n"
    "s_setprio 3\n"
    "buffer_load_dwordx2 v[30:31], v13, s[52:55], 0 offen sc1\n"
    "s_lshl_b32 s84, s93, 9\n"
    "s_add_u32 s84, s84, 0x8000\n"
    "v_lshlrev_b32 v6, 3, v32\n"
    "buffer_load_dword v34, v6, s[52:55], s84 offen sc1\n"
    "buffer_load_dword v35, v6, s[52:55], s84 offen offset:256 sc1\n"
    "s_setprio 3\n"
    "ds_write_b64 v23, v[74:75] offset:2560\n"
    "ds_write_b64 v24, v[76:77] offset:3072\n"
    "ds_write_b64 v25, v[78:79] offset:3584\n"
    "s_setprio 3\n"
    "s_waitcnt vmcnt(6)\n"
    "v_cmp_ne_u64 vcc, s[76:77], v[248:249]\n"
    "s_nop 0\n"
    "s_cbranch_vccnz .Lxs1%=\n"
    "s_mov_b32 s70, 1\n"
    "s_mov_b32 m0, s88\n"
    "s_nop 0\n"
    "buffer_load_dwordx4 v14, s[52:55], s92 offen sc1 lds\n"
    "buffer_load_dwordx4 v15, s[52:55], s92 offen offset:1024 sc1 lds\n"
    ".Lxs1%=:\n"
    "s_setprio 3\n"
    "ds_write_b64 v18, v[80:81] offset:4096\n"
    "ds_write_b64 v19, v[82:83] offset:4608\n"
    "ds_write_b64 v20, v[84:85] offset:5120\n"
    "ds_write_b64 v21, v[86:87] offset:5632\n"
    "ds_write_b64 v22, v[88:89] offset:6144\n"
    "ds_write_b64 v23, v[90:91] offset:6656\n"
    "ds_write_b64 v24, v[92:93] offset:7168\n"
    "ds_write_b64 v25, v[94:95] offset:7680\n"
    "ds_read_b128 v[64:67], v52\n"
    "ds_read_b128 v[68:71], v53\n"
    "ds_read_b128 v[72:75], v54\n"
    "ds_read_b128 v[76:79], v55\n"
    "ds_read_b128 v[80:83], v52 offset:256\n"
    "ds_read_b128 v[84:87], v53 offset:256\n"
    "ds_read_b128 v[88:91], v54 offset:256\n"
    "ds_read_b128 v[92:95], v55 offset:256\n"
    "ds_write_b64 v18, v[106:107]\n"
    "ds_write_b64 v19, v[108:109] offset:512\n"
    "ds_write_b64 v20, v[134:135] offset:1024\n"
    "s_waitcnt lgkmcnt(10)\n"
    "v_mfma_f32_16x16x32_bf16 v[146:149], v[64:67], v[154:157], 0\n"
    "s_waitcnt lgkmcnt(9)\n"
    "v_mfma_f32_16x16x32_bf16 v[146:149], v[68:71], v[158:161], v[146:149]\n"
    "ds_write_b64 v21, v[136:137] offset:1536\n"
    "s_waitcnt lgkmcnt(9)\n"
    "v_mfma_f32_16x16x32_bf16 v[146:149], v[72:75], v[162:165], v[146:149]\n"
    "ds_write_b64 v22, v[114:115] offset:2048\n"
    "s_waitcnt lgkmcnt(9)\n"
    "v_mfma_f32_16x16x32_bf16 v[146:149], v[76:79], v[166:169], v[146:149]\n"
    "ds_write_b64 v23, v[116:117] offset:2560\n"
    "s_waitcnt lgkmcnt(9)\n"
    "v_mfma_f32_16x16x32_bf16 v[146:149], v[80:83], v[170:173], v[146:149]\n"
    "ds_write_b64 v24, v[126:127] offset:3072\n"
    "s_waitcnt lgkmcnt(9)\n"
    "v_mfma_f32_16x16x32_bf16 v[146:149], v[84:87], v[174:177], v[146:149]\n"
    "ds_write_b64 v25, v[128:129] offset:3584\n"
    "s_waitcnt lgkmcnt(9)\n"
    "v_mfma_f32_16x16x32_bf16 v[146:149], v[88:91], v[178:181], v[146:149]\n"
    "ds_write_b64 v18, v[122:123] offset:4096\n"
    "s_waitcnt lgkmcnt(9)\n"
    "v_mfma_f32_16x16x32_bf16 v[146:149], v[92:95], v[182:185], v[146:149]\n"
    "ds_read_b128 v[106:109], v52\n"
    "ds_read_b128 v[110:113], v53\n"
    "ds_read_b128 v[114:117], v54\n"
    "ds_read_b128 v[118:121], v55\n"
    "ds_read_b128 v[122:125], v52 offset:256\n"
    "ds_read_b128 v[126:129], v53 offset:256\n"
    "ds_read_b128 v[130:133], v54 offset:256\n"
    "ds_read_b128 v[134:137], v55 offset:256\n"
    "ds_write2_b32 v51, v142, v143 offset0:0 offset1:2\n"
    "ds_write2_b32 v51, v144, v145 offset0:4 offset1:6\n"
    "ds_write2_b32 v56, v146, v147 offset0:0 offset1:2\n"
    "ds_write2_b32 v57, v148, v149 offset0:4 offset1:6\n"
    "s_waitcnt lgkmcnt(11)\n"
    "v_mfma_f32_16x16x32_bf16 v[150:153], v[106:109], v[154:157], 0\n"
    "s_waitcnt lgkmcnt(10)\n"
    "v_mfma_f32_16x16x32_bf16 v[150:153], v[110:113], v[158:161], v[150:153]\n"
    "s_waitcnt lgkmcnt(9)\n"
    "v_mfma_f32_16x16x32_bf16 v[150:153], v[114:117], v[162:165], v[150:153]\n"
    "s_waitcnt lgkmcnt(8)\n"
    "v_mfma_f32_16x16x32_bf16 v[150:153], v[118:121], v[166:169], v[150:153]\n"
    "s_waitcnt lgkmcnt(7)\n"
    "v_mfma_f32_16x16x32_bf16 v[150:153], v[122:125], v[170:173], v[150:153]\n"
    "s_waitcnt lgkmcnt(6)\n"
    "v_mfma_f32_16x16x32_bf16 v[150:153], v[126:129], v[174:177], v[150:153]\n"
    "s_waitcnt lgkmcnt(5)\n"
    "v_mfma_f32_16x16x32_bf16 v[150:153], v[130:133], v[178:181], v[150:153]\n"
    "s_waitcnt lgkmcnt(4)\n"
    "v_mfma_f32_16x16x32_bf16 v[150:153], v[134:137], v[182:185], v[150:153]\n"
    "s_setprio 3\n"
    "s_cmp_eq_u32 s70, 1\n"
    "s_cbranch_scc1 .Lxs4%=\n"
    "s_waitcnt vmcnt(2)\n"
    "v_cmp_ne_u64 vcc, s[76:77], v[30:31]\n"
    "s_nop 0\n"
    "s_cbranch_vccnz .Lxs4%=\n"
    "s_mov_b32 s70, 1\n"
    "s_mov_b32 m0, s88\n"
    "s_nop 0\n"
    "buffer_load_dwordx4 v14, s[52:55], s92 offen sc1 lds\n"
    "buffer_load_dwordx4 v15, s[52:55], s92 offen offset:1024 sc1 lds\n"
    ".Lxs4%=:\n"
    "s_setprio 3\n"
    "ds_write2_b32 v58, v150, v151 offset0:0 offset1:34\n"
    "ds_write2_b32 v58, v152, v153 offset0:68 offset1:102\n"
    "s_mov_b64 exec, 1\n"
    "ds_write_b32 v96, v98 offset:112\n"
    "s_mov_b64 exec, -1\n"
    "ds_write_b128 v59, v[60:63]\n"
    "ds_read_b32 v3, v12 offset:112\n"
    "s_waitcnt lgkmcnt(0)\n"
    "v_cmp_eq_u32 vcc, 0, v3\n"
    "s_nop 0\n"
    "s_cbranch_vccnz .Lslowt0%=\n"
    ".Lokt0%=:\n"
    "ds_read_b64 v[186:187], v220\n"
    "ds_read_b64 v[188:189], v221\n"
    "ds_read_b64 v[190:191], v222\n"
    "ds_read_b64 v[192:193], v223\n"
    "ds_read_b64 v[194:195], v224\n"
    "ds_read_b64 v[196:197], v225\n"
    "ds_read_b64 v[198:199], v226\n"
    "ds_read_b64 v[200:201], v227\n"
    "ds_read_b64 v[202:203], v228\n"
    "ds_read_b64 v[204:205], v229\n"
    "ds_read_b64 v[206:207], v230\n"
    "ds_read_b64 v[208:209], v231\n"
    "ds_read_b64 v[210:211], v232\n"
    "ds_read_b64 v[212:213], v233\n"
    "ds_read_b64 v[214:215], v234\n"
    "ds_read_b64 v[216:217], v235\n"
    "ds_read_b64 v[64:65], v236\n"
    "s_setprio 3\n"
    "s_cmp_eq_u32 s70, 1\n"
    "s_cbranch_scc1 .Lxs5%=\n"
    "buffer_load_dwordx2 v[30:31], v13, s[52:55], 0 offen sc1\n"
    ".Lxs5%=:\n"
    "s_setprio 3\n"
    "s_mov_b32 exec_lo, 0\n"
    "s_mov_b32 exec_hi, 0xff\n"
    "ds_write_b128 v218, v[60:63]\n"
    "s_mov_b64 exec, -1\n"
    "s_waitcnt lgkmcnt(14)\n"
    "v_add_f32 v186, v186, v187\n"
    "v_add_f32 v188, v188, v189\n"
    "v_add_f32 v190, v190, v191\n"
    "v_add_f32 v192, v192, v193\n"
    "s_waitcnt lgkmcnt(13)\n"
    "v_add_f32 v194, v194, v195\n"
    "s_waitcnt lgkmcnt(12)\n"
    "v_add_f32 v196, v196, v197\n"
    "s_waitcnt lgkmcnt(11)\n"
    "v_add_f32 v198, v198, v199\n"
    "s_waitcnt lgkmcnt(10)\n"
    "v_add_f32 v200, v200, v201\n"
    "s_waitcnt lgkmcnt(9)\n"
    "v_add_f32 v202, v202, v203\n"
    "v_cvt_pk_bf16_f32 v154, v186, v188\n"
    "v_cvt_pk_bf16_f32 v155, v190, v192\n"
    "v_cvt_pk_bf16_f32 v156, v194, v196\n"
    "v_cvt_pk_bf16_f32 v157, v198, v200\n"
    "ds_write_b128 v10, v[154:157]\n"
    "v_cvt_pk_bf16_f32 v158, v188, v190\n"
    "v_cvt_pk_bf16_f32 v159, v192, v194\n"
    "v_cvt_pk_bf16_f32 v160, v196, v198\n"
    "v_cvt_pk_bf16_f32 v161, v200, v202\n"
    "ds_write_b128 v10, v[158:161] offset:4160\n"
    "s_waitcnt lgkmcnt(10)\n"
    "v_add_f32 v204, v204, v205\n"
    "s_waitcnt lgkmcnt(9)\n"
    "v_add_f32 v206, v206, v207\n"
    "s_waitcnt lgkmcnt(8)\n"
    "v_add_f32 v208, v208, v209\n"
    "s_waitcnt lgkmcnt(7)\n"
    "v_add_f32 v210, v210, v211\n"
    "s_waitcnt lgkmcnt(6)\n"
    "v_add_f32 v212, v212, v213\n"
    "s_waitcnt lgkmcnt(5)\n"
    "v_add_f32 v214, v214, v215\n"
    "s_waitcnt lgkmcnt(4)\n"
    "v_add_f32 v216, v216, v217\n"
    "s_waitcnt lgkmcnt(3)\n"
    "v_add_f32 v64, v64, v65\n"
    "v_cvt_pk_bf16_f32 v154, v202, v204\n"
    "v_cvt_pk_bf16_f32 v155, v206, v208\n"
    "v_cvt_pk_bf16_f32 v156, v210, v212\n"
    "v_cvt_pk_bf16_f32 v157, v214, v216\n"
    "ds_write_b128 v10, v[154:157] offset:16\n"
    "v_cvt_pk_bf16_f32 v158, v204, v206\n"
    "v_cvt_pk_bf16_f32 v159, v208, v210\n"
    "v_cvt_pk_bf16_f32 v160, v212, v214\n"
    "v_cvt_pk_bf16_f32 v161, v216, v64\n"
    "ds_write_b128 v10, v[158:161] offset:4176\n"
    "s_setprio 3\n"
    "s_cmp_eq_u32 s70, 1\n"
    "s_cbranch_scc1 .Lxs9%=\n"
    "s_waitcnt vmcnt(0)\n"
    "v_cmp_ne_u64 vcc, s[76:77], v[30:31]\n"
    "s_nop 0\n"
    "s_cbranch_vccnz .Lslowx%=\n"
    ".Lokx%=:\n"
    "s_mov_b32 m0, s88\n"
    "s_nop 0\n"
    "buffer_load_dwordx4 v14, s[52:55], s92 offen sc1 lds\n"
    "buffer_load_dwordx4 v15, s[52:55], s92 offen offset:1024 sc1 lds\n"
    ".Lxs9%=:\n"
    "s_setprio 3\n"
    "s_waitcnt vmcnt(0)\n"
    "s_mov_b64 exec, 1\n"
    "ds_write_b32 v96, v98 offset:80\n"
    "s_mov_b64 exec, -1\n"
    "ds_read_b32 v3, v97 offset:80\n"
    "s_waitcnt lgkmcnt(0)\n"
    "v_cmp_eq_u32 vcc, 0, v3\n"
    "s_nop 0\n"
    "s_cbranch_vccnz .Lslowt2%=\n"
    ".Lokt2%=:\n"
    "s_setprio 1\n"
    "ds_read2_b32 v[100:101], v29 offset0:176 offset1:177\n"
    "ds_read2_b32 v[102:103], v29 offset0:178 offset1:179\n"
    "ds_read_b128 v[164:167], v11 offset:192\n"
    "ds_read2_b32 v[104:105], v29 offset0:48 offset1:49\n"
    "ds_read2_b32 v[106:107], v29 offset0:50 offset1:51\n"
    "ds_read_b128 v[168:171], v11 offset:704\n"
    "ds_read2_b32 v[108:109], v28 offset0:176 offset1:177\n"
    "ds_read2_b32 v[110:111], v28 offset0:178 offset1:179\n"
    "ds_read_b128 v[172:175], v11 offset:1216\n"
    "s_waitcnt lgkmcnt(6)\n"
    "v_mfma_f32_16x16x32_bf16 v[52:55], v[100:103], v[164:167], 0\n"
    "ds_read2_b32 v[112:113], v28 offset0:48 offset1:49\n"
    "ds_read2_b32 v[114:115], v28 offset0:50 offset1:51\n"
    "s_waitcnt lgkmcnt(5)\n"
    "v_mfma_f32_16x16x32_bf16 v[56:59], v[100:103], v[168:171], 0\n"
    "ds_read_b128 v[176:179], v11 offset:1728\n"
    "v_mfma_f32_16x16x32_bf16 v[56:59], v[104:107], v[164:167], v[56:59]\n"
    "ds_read2_b32 v[116:117], v27 offset0:176 offset1:177\n"
    "ds_read2_b32 v[118:119], v27 offset0:178 offset1:179\n"
    "v_mfma_f32_16x16x32_bf16 v[60:63], v[104:107], v[168:171], 0\n"
    "ds_read_b128 v[180:183], v11 offset:2240\n"
    "s_waitcnt lgkmcnt(6)\n"
    "v_mfma_f32_16x16x32_bf16 v[60:63], v[100:103], v[172:175], v[60:63]\n"
    "ds_read2_b32 v[120:121], v27 offset0:48 offset1:49\n"
    "ds_read2_b32 v[122:123], v27 offset0:50 offset1:51\n"
    "v_mfma_f32_16x16x32_bf16 v[64:67], v[104:107], v[172:175], 0\n"
    "ds_read_b128 v[184:187], v11 offset:2752\n"
    "v_mfma_f32_16x16x32_bf16 v[60:63], v[108:111], v[164:167], v[60:63]\n"
    "ds_read2_b32 v[124:125], v26 offset0:176 offset1:177\n"
    "ds_read2_b32 v[126:127], v26 offset0:178 offset1:179\n"
    "v_mfma_f32_16x16x32_bf16 v[64:67], v[108:111], v[168:171], v[64:67]\n"
    "ds_read_b128 v[188:191], v11 offset:3264\n"
    "v_mfma_f32_16x16x32_bf16 v[68:71], v[108:111], v[172:175], 0\n"
    "ds_read2_b32 v[128:129], v26 offset0:48 offset1:49\n"
    "ds_read2_b32 v[130:131], v26 offset0:50 offset1:51\n"
    "s_waitcnt lgkmcnt(11)\n"
    "v_mfma_f32_16x16x32_bf16 v[64:67], v[100:103], v[176:179], v[64:67]\n"
    "ds_read_b128 v[192:195], v11 offset:3776\n"
    "v_mfma_f32_16x16x32_bf16 v[68:71], v[104:107], v[176:179], v[68:71]\n"
    "ds_read2_b32 v[132:133], v29 offset0:160 offset1:161\n"
    "ds_read2_b32 v[134:135], v29 offset0:162 offset1:163\n"
    "v_mfma_f32_16x16x32_bf16 v[72:75], v[108:111], v[176:179], 0\n"
    "ds_read_b128 v[196:199], v11 offset:128\n"
    "v_mfma_f32_16x16x32_bf16 v[64:67], v[112:115], v[164:167], v[64:67]\n"
    "ds_read2_b32 v[136:137], v29 offset0:32 offset1:33\n"
    "v_mfma_f32_16x16x32_bf16 v[68:71], v[112:115], v[168:171], v[68:71]\n"
    "ds_read2_b32 v[138:139], v29 offset0:34 offset1:35\n"
    "v_mfma_f32_16x16x32_bf16 v[72:75], v[112:115], v[172:175], v[72:75]\n"
    "ds_read_b128 v[200:203], v11 offset:640\n"
    "v_mfma_f32_16x16x32_bf16 v[76:79], v[112:115], v[176:179], 0\n"
    "ds_read2_b32 v[140:141], v28 offset0:160 offset1:161\n"
    "s_waitcnt lgkmcnt(14)\n"
    "v_mfma_f32_16x16x32_bf16 v[68:71], v[100:103], v[180:183], v[68:71]\n"
    "ds_read2_b32 v[142:143], v28 offset0:162 offset1:163\n"
    "v_mfma_f32_16x16x32_bf16 v[72:75], v[104:107], v[180:183], v[72:75]\n"
    "ds_read_b128 v[204:207], v11 offset:1152\n"
    "v_mfma_f32_16x16x32_bf16 v[76:79], v[108:111], v[180:183], v[76:79]\n"
    "ds_read2_b32 v[144:145], v28 offset0:32 offset1:33\n"
    "v_mfma_f32_16x16x32_bf16 v[80:83], v[112:115], v[180:183], 0\n"
    "ds_read2_b32 v[146:147], v28 offset0:34 offset1:35\n"
    "v_mfma_f32_16x16x32_bf16 v[68:71], v[116:119], v[164:167], v[68:71]\n"
    "ds_read_b128 v[208:211], v11 offset:1664\n"
    "v_mfma_f32_16x16x32_bf16 v[72:75], v[116:119], v[168:171], v[72:75]\n"
    "ds_read2_b32 v[148:149], v27 offset0:160 offset1:161\n"
    "ds_read2_b32 v[150:151], v27 offset0:162 offset1:163\n"
    "v_mfma_f32_16x16x32_bf16 v[76:79], v[116:119], v[172:175], v[76:79]\n"
    "ds_read_b128 v[212:215], v11 offset:2176\n"
    "v_mfma_f32_16x16x32_bf16 v[80:83], v[116:119], v[176:179], v[80:83]\n"
    "ds_read2_b32 v[152:153], v27 offset0:32 offset1:33\n"
    "s_waitcnt lgkmcnt(14)\n"
    "v_mfma_f32_16x16x32_bf16 v[72:75], v[100:103], v[184:187], v[72:75]\n"
    "ds_read2_b32 v[154:155], v27 offset0:34 offset1:35\n"
    "v_mfma_f32_16x16x32_bf16 v[76:79], v[104:107], v[184:187], v[76:79]\n"
    "ds_read_b128 v[216:219], v11 offset:2688\n"
    "v_mfma_f32_16x16x32_bf16 v[80:83], v[108:111], v[184:187], v[80:83]\n"
    "ds_read2_b32 v[156:157], v26 offset0:160 offset1:161\n"
    "v_mfma_f32_16x16x32_bf16 v[72:75], v[120:123], v[164:167], v[72:75]\n"
    "ds_read2_b32 v[158:159], v26 offset0:162 offset1:163\n"
    "v_mfma_f32_16x16x32_bf16 v[76:79], v[120:123], v[168:171], v[76:79]\n"
    "ds_read_b128 v[220:223], v11 offset:3200\n"
    "v_mfma_f32_16x16x32_bf16 v[80:83], v[120:123], v[172:175], v[80:83]\n"
    "ds_read2_b32 v[160:161], v26 offset0:32 offset1:33\n"
    "v_mfma_f32_16x16x32_bf16 v[76:79], v[100:103], v[188:191], v[76:79]\n"
    "ds_read2_b32 v[162:163], v26 offset0:34 offset1:35\n"
    "v_mfma_f32_16x16x32_bf16 v[80:83], v[104:107], v[188:191], v[80:83]\n"
    "ds_read_b128 v[224:227], v11 offset:3712\n"
    "v_mfma_f32_16x16x32_bf16 v[76:79], v[124:127], v[164:167], v[76:79]\n"
    "v_mfma_f32_16x16x32_bf16 v[80:83], v[124:127], v[168:171], v[80:83]\n"
    "v_mfma_f32_16x16x32_bf16 v[80:83], v[100:103], v[192:195], v[80:83]\n"
    "v_mfma_f32_16x16x32_bf16 v[80:83], v[128:131], v[164:167], v[80:83]\n"
    "s_waitcnt lgkmcnt(14)\n"
    "v_mfma_f32_16x16x32_bf16 v[52:55], v[132:135], v[196:199], v[52:55]\n"
    "ds_read2_b32 v[100:101], v29 offset0:144 offset1:145\n"
    "ds_read2_b32 v[102:103], v29 offset0:146 offset1:147\n"
    "v_mfma_f32_16x16x32_bf16 v[56:59], v[132:135], v[200:203], v[56:59]\n"
    "ds_read_b128 v[164:167], v11 offset:64\n"
    "v_mfma_f32_16x16x32_bf16 v[56:59], v[136:139], v[196:199], v[56:59]\n"
    "ds_read2_b32 v[104:105], v29 offset0:16 offset1:17\n"
    "v_mfma_f32_16x16x32_bf16 v[60:63], v[136:139], v[200:203], v[60:63]\n"
    "ds_read2_b32 v[106:107], v29 offset0:18 offset1:19\n"
    "v_mfma_f32_16x16x32_bf16 v[60:63], v[132:135], v[204:207], v[60:63]\n"
    "ds_read_b128 v[168:171], v11 offset:576\n"
    "v_mfma_f32_16x16x32_bf16 v[64:67], v[136:139], v[204:207], v[64:67]\n"
    "ds_read2_b32 v[108:109], v28 offset0:144 offset1:145\n"
    "v_mfma_f32_16x16x32_bf16 v[60:63], v[140:143], v[196:199], v[60:63]\n"
    "ds_read2_b32 v[110:111], v28 offset0:146 offset1:147\n"
    "v_mfma_f32_16x16x32_bf16 v[64:67], v[140:143], v[200:203], v[64:67]\n"
    "ds_read_b128 v[172:175], v11 offset:1088\n"
    "v_mfma_f32_16x16x32_bf16 v[68:71], v[140:143], v[204:207], v[68:71]\n"
    "ds_read2_b32 v[112:113], v28 offset0:16 offset1:17\n"
    "s_waitcnt lgkmcnt(14)\n"
    "v_mfma_f32_16x16x32_bf16 v[64:67], v[132:135], v[208:211], v[64:67]\n"
    "ds_read2_b32 v[114:115], v28 offset0:18 offset1:19\n"
    "v_mfma_f32_16x16x32_bf16 v[68:71], v[136:139], v[208:211], v[68:71]\n"
    "ds_read_b128 v[176:179], v11 offset:1600\n"
    "v_mfma_f32_16x16x32_bf16 v[72:75], v[140:143], v[208:211], v[72:75]\n"
    "ds_read2_b32 v[116:117], v27 offset0:144 offset1:145\n"
    "v_mfma_f32_16x16x32_bf16 v[64:67], v[144:147], v[196:199], v[64:67]\n"
    "ds_read2_b32 v[118:119], v27 offset0:146 offset1:147\n"
    "v_mfma_f32_16x16x32_bf16 v[68:71], v[144:147], v[200:203], v[68:71]\n"
    "ds_read_b128 v[180:183], v11 offset:2112\n"
    "v_mfma_f32_16x16x32_bf16 v[72:75], v[144:147], v[204:207], v[72:75]\n"
    "ds_read2_b32 v[120:121], v27 offset0:16 offset1:17\n"
    "v_mfma_f32_16x16x32_bf16 v[76:79], v[144:147], v[208:211], v[76:79]\n"
    "ds_read2_b32 v[122:123], v27 offset0:18 offset1:19\n"
    "v_mfma_f32_16x16x32_bf16 v[68:71], v[132:135], v[212:215], v[68:71]\n"
    "ds_read_b128 v[184:187], v11 offset:2624\n"
    "v_mfma_f32_16x16x32_bf16 v[72:75], v[136:139], v[212:215], v[72:75]\n"
    "ds_read2_b32 v[124:125], v26 offset0:144 offset1:145\n"
    "v_mfma_f32_16x16x32_bf16 v[76:79], v[140:143], v[212:215], v[76:79]\n"
    "ds_read2_b32 v[126:127], v26 offset0:146 offset1:147\n"
    "ds_read_b128 v[188:191], v11 offset:3136\n"
    "v_mfma_f32_16x16x32_bf16 v[80:83], v[144:147], v[212:215], v[80:83]\n"
    "ds_read2_b32 v[128:129], v26 offset0:16 offset1:17\n"
    "v_mfma_f32_16x16x32_bf16 v[68:71], v[148:151], v[196:199], v[68:71]\n"
    "ds_read2_b32 v[130:131], v26 offset0:18 offset1:19\n"
    "v_mfma_f32_16x16x32_bf16 v[72:75], v[148:151], v[200:203], v[72:75]\n"
    "ds_read_b128 v[192:195], v11 offset:3648\n"
    "v_mfma_f32_16x16x32_bf16 v[76:79], v[148:151], v[204:207], v[76:79]\n"
    "s_add_u32 s74, s89, 0x22600\n"
    "v_mfma_f32_16x16x32_bf16 v[80:83], v[148:151], v[208:211], v[80:83]\n"
    "v_add_u32 v14, s74, v2\n"
    "v_mfma_f32_16x16x32_bf16 v[72:75], v[132:135], v[216:219], v[72:75]\n"
    "s_xor_b32 s74, s89, 0x1000\n"
    "v_mfma_f32_16x16x32_bf16 v[76:79], v[136:139], v[216:219], v[76:79]\n"
    "s_add_u32 s74, s74, 0x22600\n"
    "v_mfma_f32_16x16x32_bf16 v[80:83], v[140:143], v[216:219], v[80:83]\n"
    "v_add_u32 v15, s74, v2\n"
    "v_mfma_f32_16x16x32_bf16 v[72:75], v[152:155], v[196:199], v[72:75]\n"
    "v_and_b32 v3, 1, v49\n"
    "v_mfma_f32_16x16x32_bf16 v[76:79], v[152:155], v[200:203], v[76:79]\n"
    "v_lshrrev_b32 v4, 2, v50\n"
    "v_mfma_f32_16x16x32_bf16 v[80:83], v[152:155], v[204:207], v[80:83]\n"
    "v_lshl_add_u32 v3, v3, 2, v4\n"
    "s_waitcnt lgkmcnt(14)\n"
    "v_mfma_f32_16x16x32_bf16 v[76:79], v[132:135], v[220:223], v[76:79]\n"
    "v_mul_u32_u24 v3, 0x2000, v3\n"
    "v_mfma_f32_16x16x32_bf16 v[80:83], v[136:139], v[220:223], v[80:83]\n"
    "v_and_b32 v4, 3, v1\n"
    "v_mfma_f32_16x16x32_bf16 v[76:79], v[156:159], v[196:199], v[76:79]\n"
    "v_lshl_add_u32 v4, v17, 2, v4\n"
    "v_mfma_f32_16x16x32_bf16 v[80:83], v[156:159], v[200:203], v[80:83]\n"
    "v_lshl_add_u32 v10, v4, 4, v3\n"
    "v_mfma_f32_16x16x32_bf16 v[80:83], v[132:135], v[224:227], v[80:83]\n"
    "s_lshl_b32 s74, s82, 1\n"
    "v_mfma_f32_16x16x32_bf16 v[80:83], v[160:163], v[196:199], v[80:83]\n"
    "s_lshr_b32 s75, s69, 1\n"
    "v_mfma_f32_16x16x32_bf16 v[52:55], v[100:103], v[164:167], v[52:55]\n"
    "ds_read2_b32 v[132:133], v29 offset0:128 offset1:129\n"
    "ds_read2_b32 v[134:135], v29 offset0:130 offset1:131\n"
    "v_mfma_f32_16x16x32_bf16 v[56:59], v[100:103], v[168:171], v[56:59]\n"
    "ds_read_b128 v[196:199], v11\n"
    "v_mfma_f32_16x16x32_bf16 v[56:59], v[104:107], v[164:167], v[56:59]\n"
    "ds_read2_b32 v[136:137], v29 offset0:0 offset1:1\n"
    "v_mfma_f32_16x16x32_bf16 v[60:63], v[104:107], v[168:171], v[60:63]\n"
    "ds_read2_b32 v[138:139], v29 offset0:2 offset1:3\n"
    "v_mfma_f32_16x16x32_bf16 v[60:63], v[100:103], v[172:175], v[60:63]\n"
    "ds_read_b128 v[200:203], v11 offset:512\n"
    "v_mfma_f32_16x16x32_bf16 v[64:67], v[104:107], v[172:175], v[64:67]\n"
    "ds_read2_b32 v[140:141], v28 offset0:128 offset1:129\n"
    "v_mfma_f32_16x16x32_bf16 v[60:63], v[108:111], v[164:167], v[60:63]\n"
    "ds_read2_b32 v[142:143], v28 offset0:130 offset1:131\n"
    "v_mfma_f32_16x16x32_bf16 v[64:67], v[108:111], v[168:171], v[64:67]\n"
    "ds_read_b128 v[204:207], v11 offset:1024\n"
    "v_mfma_f32_16x16x32_bf16 v[68:71], v[108:111], v[172:175], v[68:71]\n"
    "ds_read2_b32 v[144:145], v28 offset0:0 offset1:1\n"
    "s_waitcnt lgkmcnt(14)\n"
    "v_mfma_f32_16x16x32_bf16 v[64:67], v[100:103], v[176:179], v[64:67]\n"
    "ds_read2_b32 v[146:147], v28 offset0:2 offset1:3\n"
    "v_mfma_f32_16x16x32_bf16 v[68:71], v[104:107], v[176:179], v[68:71]\n"
    "ds_read_b128 v[208:211], v11 offset:1536\n"
    "v_mfma_f32_16x16x32_bf16 v[72:75], v[108:111], v[176:179], v[72:75]\n"
    "ds_read2_b32 v[148:149], v27 offset0:128 offset1:129\n"
    "v_mfma_f32_16x16x32_bf16 v[64:67], v[112:115], v[164:167], v[64:67]\n"
    "ds_read2_b32 v[150:151], v27 offset0:130 offset1:131\n"
    "v_mfma_f32_16x16x32_bf16 v[68:71], v[112:115], v[168:171], v[68:71]\n"
    "ds_read_b128 v[212:215], v11 offset:2048\n"
    "v_mfma_f32_16x16x32_bf16 v[72:75], v[112:115], v[172:175], v[72:75]\n"
    "ds_read2_b32 v[152:153], v27 offset0:0 offset1:1\n"
    "v_mfma_f32_16x16x32_bf16 v[76:79], v[112:115], v[176:179], v[76:79]\n"
    "ds_read2_b32 v[154:155], v27 offset0:2 offset1:3\n"
    "v_mfma_f32_16x16x32_bf16 v[68:71], v[100:103], v[180:183], v[68:71]\n"
    "ds_read_b128 v[216:219], v11 offset:2560\n"
    "v_mfma_f32_16x16x32_bf16 v[72:75], v[104:107], v[180:183], v[72:75]\n"
    "ds_read2_b32 v[156:157], v26 offset0:128 offset1:129\n"
    "v_mfma_f32_16x16x32_bf16 v[76:79], v[108:111], v[180:183], v[76:79]\n"
    "ds_read2_b32 v[158:159], v26 offset0:130 offset1:131\n"
    "ds_read_b128 v[220:223], v11 offset:3072\n"
    "v_mfma_f32_16x16x32_bf16 v[80:83], v[112:115], v[180:183], v[80:83]\n"
    "ds_read2_b32 v[160:161], v26 offset0:0 offset1:1\n"
    "v_mfma_f32_16x16x32_bf16 v[68:71], v[116:119], v[164:167], v[68:71]\n"
    "ds_read2_b32 v[162:163], v26 offset0:2 offset1:3\n"
    "v_mfma_f32_16x16x32_bf16 v[72:75], v[116:119], v[168:171], v[72:75]\n"
    "ds_read_b128 v[224:227], v11 offset:3584\n"
    "v_mfma_f32_16x16x32_bf16 v[76:79], v[116:119], v[172:175], v[76:79]\n"
    "s_add_u32 s74, s74, s75\n"
    "v_mfma_f32_16x16x32_bf16 v[80:83], v[116:119], v[176:179], v[80:83]\n"
    "s_and_b32 s72, s69, 1\n"
    "v_mfma_f32_16x16x32_bf16 v[72:75], v[100:103], v[184:187], v[72:75]\n"
    "s_lshl_b32 s75, s72, 9\n"
    "v_mfma_f32_16x16x32_bf16 v[76:79], v[104:107], v[184:187], v[76:79]\n"
    "s_add_u32 s75, s75, s74\n"
    "v_mfma_f32_16x16x32_bf16 v[80:83], v[108:111], v[184:187], v[80:83]\n"
    "s_lshl_b32 s75, s75, 7\n"
    "v_mfma_f32_16x16x32_bf16 v[72:75], v[120:123], v[164:167], v[72:75]\n"
    "s_add_u32 s85, s75, 0x50000\n"
    "v_mfma_f32_16x16x32_bf16 v[76:79], v[120:123], v[168:171], v[76:79]\n"
    "s_lshr_b32 s75, s74, 6\n"
    "v_mfma_f32_16x16x32_bf16 v[80:83], v[120:123], v[172:175], v[80:83]\n"
    "s_mul_i32 s75, s75, 0x40000\n"
    "s_waitcnt lgkmcnt(14)\n"
    "v_mfma_f32_16x16x32_bf16 v[76:79], v[100:103], v[188:191], v[76:79]\n"
    "s_and_b32 s74, s74, 63\n"
    "v_mfma_f32_16x16x32_bf16 v[80:83], v[104:107], v[188:191], v[80:83]\n"
    "s_lshl_b32 s74, s74, 7\n"
    "v_mfma_f32_16x16x32_bf16 v[76:79], v[124:127], v[164:167], v[76:79]\n"
    "s_add_u32 s74, s74, s75\n"
    "v_mfma_f32_16x16x32_bf16 v[80:83], v[124:127], v[168:171], v[80:83]\n"
    "s_mul_i32 s75, s72, 0x20000\n"
    "v_mfma_f32_16x16x32_bf16 v[80:83], v[100:103], v[192:195], v[80:83]\n"
    "s_add_u32 s74, s74, s75\n"
    "v_mfma_f32_16x16x32_bf16 v[80:83], v[128:131], v[164:167], v[80:83]\n"
    "s_add_u32 s87, s74, 0x250000\n"
    "s_bitcmp1_b32 s69, 0\n"
    "s_cbranch_scc1 .Lce1%=\n"
    ".Lcut4%=:\n"
    "v_mfma_f32_16x16x32_bf16 v[68:71], v[140:143], v[204:207], v[68:71]\n"
    "s_waitcnt lgkmcnt(12)\n"
    "v_mfma_f32_16x16x32_bf16 v[68:71], v[136:139], v[208:211], v[68:71]\n"
    "v_mfma_f32_16x16x32_bf16 v[72:75], v[140:143], v[208:211], v[72:75]\n"
    "v_mfma_f32_16x16x32_bf16 v[68:71], v[144:147], v[200:203], v[68:71]\n"
    "v_mfma_f32_16x16x32_bf16 v[72:75], v[144:147], v[204:207], v[72:75]\n"
    "v_mfma_f32_16x16x32_bf16 v[76:79], v[144:147], v[208:211], v[76:79]\n"
    "s_waitcnt lgkmcnt(9)\n"
    "v_mfma_f32_16x16x32_bf16 v[68:71], v[132:135], v[212:215], v[68:71]\n"
    "v_mfma_f32_16x16x32_bf16 v[72:75], v[136:139], v[212:215], v[72:75]\n"
    "v_mfma_f32_16x16x32_bf16 v[76:79], v[140:143], v[212:215], v[76:79]\n"
    "v_mfma_f32_16x16x32_bf16 v[80:83], v[144:147], v[212:215], v[80:83]\n"
    "v_mfma_f32_16x16x32_bf16 v[68:71], v[148:151], v[196:199], v[68:71]\n"
    "v_mfma_f32_16x16x32_bf16 v[72:75], v[148:151], v[200:203], v[72:75]\n"
    "v_mfma_f32_16x16x32_bf16 v[76:79], v[148:151], v[204:207], v[76:79]\n"
    "v_mfma_f32_16x16x32_bf16 v[80:83], v[148:151], v[208:211], v[80:83]\n"
    "s_waitcnt lgkmcnt(6)\n"
    "v_mfma_f32_16x16x32_bf16 v[72:75], v[132:135], v[216:219], v[72:75]\n"
    "v_mfma_f32_16x16x32_bf16 v[76:79], v[136:139], v[216:219], v[76:79]\n"
    "v_mfma_f32_16x16x32_bf16 v[80:83], v[140:143], v[216:219], v[80:83]\n"
    "v_mfma_f32_16x16x32_bf16 v[72:75], v[152:155], v[196:199], v[72:75]\n"
    "v_mfma_f32_16x16x32_bf16 v[76:79], v[152:155], v[200:203], v[76:79]\n"
    "v_mfma_f32_16x16x32_bf16 v[80:83], v[152:155], v[204:207], v[80:83]\n"
    "s_waitcnt lgkmcnt(3)\n"
    "v_mfma_f32_16x16x32_bf16 v[76:79], v[132:135], v[220:223], v[76:79]\n"
    "v_mfma_f32_16x16x32_bf16 v[80:83], v[136:139], v[220:223], v[80:83]\n"
    "v_mfma_f32_16x16x32_bf16 v[76:79], v[156:159], v[196:199], v[76:79]\n"
    "v_mfma_f32_16x16x32_bf16 v[80:83], v[156:159], v[200:203], v[80:83]\n"
    "s_waitcnt lgkmcnt(0)\n"
    "v_mfma_f32_16x16x32_bf16 v[80:83], v[132:135], v[224:227], v[80:83]\n"
    "v_mfma_f32_16x16x32_bf16 v[80:83], v[160:163], v[196:199], v[80:83]\n"
    "v_mfma_f32_16x16x32_bf16 v[52:55], v[132:135], v[196:199], v[52:55]\n"
    "v_mfma_f32_16x16x32_bf16 v[56:59], v[132:135], v[200:203], v[56:59]\n"
    "v_mfma_f32_16x16x32_bf16 v[56:59], v[136:139], v[196:199], v[56:59]\n"
    "ds_write_b128 v14, v[68:71]\n"
    "v_mfma_f32_16x16x32_bf16 v[60:63], v[136:139], v[200:203], v[60:63]\n"
    "ds_write_b128 v14, v[72:75] offset:1024\n"
    "v_mfma_f32_16x16x32_bf16 v[60:63], v[132:135], v[204:207], v[60:63]\n"
    "v_mfma_f32_16x16x32_bf16 v[64:67], v[136:139], v[204:207], v[64:67]\n"
    "ds_write_b128 v14, v[76:79] offset:2048\n"
    "v_mfma_f32_16x16x32_bf16 v[60:63], v[140:143], v[196:199], v[60:63]\n"
    "ds_write_b128 v14, v[80:83] offset:3072\n"
    "v_mfma_f32_16x16x32_bf16 v[64:67], v[140:143], v[200:203], v[64:67]\n"
    "v_mfma_f32_16x16x32_bf16 v[64:67], v[132:135], v[208:211], v[64:67]\n"
    "s_mov_b64 exec, 1\n"
    "ds_write_b32 v96, v98 offset:32\n"
    "s_mov_b64 exec, -1\n"
    "v_mfma_f32_16x16x32_bf16 v[64:67], v[144:147], v[196:199], v[64:67]\n"
    "s_setprio 1\n"
    "ds_read_b32 v3, v97 offset:32\n"
    "s_waitcnt lgkmcnt(0)\n"
    "v_cmp_eq_u32 vcc, 0, v3\n"
    "s_nop 0\n"
    "s_cbranch_vccnz .Lslowt30%=\n"
    ".Lokt30%=:\n"
    "ds_read_b128 v[164:167], v15\n"
    "ds_read_b128 v[168:171], v15 offset:1024\n"
    "ds_read_b128 v[172:175], v15 offset:2048\n"
    "ds_read_b128 v[176:179], v15 offset:3072\n"
    "s_waitcnt lgkmcnt(3)\n"
    "v_add_f32 v164, v164, v52\n"
    "v_add_f32 v165, v165, v53\n"
    "v_add_f32 v166, v166, v54\n"
    "v_add_f32 v167, v167, v55\n"
    "v_cvt_pk_bf16_f32 v100, v164, v165\n"
    "v_cvt_pk_bf16_f32 v101, v166, v167\n"
    "s_waitcnt lgkmcnt(2)\n"
    "v_add_f32 v168, v168, v56\n"
    "v_add_f32 v169, v169, v57\n"
    "v_add_f32 v170, v170, v58\n"
    "v_add_f32 v171, v171, v59\n"
    "v_cvt_pk_bf16_f32 v102, v168, v169\n"
    "v_cvt_pk_bf16_f32 v103, v170, v171\n"
    "s_nop 0\n"
    "v_permlane16_swap_b32 v100, v102\n"
    "v_permlane16_swap_b32 v101, v103\n"
    "buffer_store_dwordx4 v[100:103], v10, s[52:55], s87 offen sc1\n"
    "s_add_u32 s87, s87, 0x10000\n"
    "s_waitcnt lgkmcnt(1)\n"
    "v_add_f32 v172, v172, v60\n"
    "v_add_f32 v173, v173, v61\n"
    "v_add_f32 v174, v174, v62\n"
    "v_add_f32 v175, v175, v63\n"
    "v_cvt_pk_bf16_f32 v104, v172, v173\n"
    "v_cvt_pk_bf16_f32 v105, v174, v175\n"
    "s_waitcnt lgkmcnt(0)\n"
    "v_add_f32 v176, v176, v64\n"
    "v_add_f32 v177, v177, v65\n"
    "v_add_f32 v178, v178, v66\n"
    "v_add_f32 v179, v179, v67\n"
    "v_cvt_pk_bf16_f32 v106, v176, v177\n"
    "v_cvt_pk_bf16_f32 v107, v178, v179\n"
    ".Lcej%=:\n"
    "s_nop 0\n"
    "v_permlane16_swap_b32 v104, v106\n"
    "v_permlane16_swap_b32 v105, v107\n"
    "buffer_store_dwordx4 v[104:107], v10, s[52:55], s87 offen sc1\n"
    "s_add_u32 s87, s87, 0x10000\n"
    "s_setprio 0\n"
    "v_and_b32 v34, 0xffff0000, v34\n"
    "v_mul_f32 v34, v34, v34\n"
    "v_rcp_f32 v34, v34\n"
    "v_and_b32 v35, 0xffff0000, v35\n"
    "v_mul_f32 v35, v35, v35\n"
    "v_rcp_f32 v35, v35\n"
    "v_lshlrev_b32 v3, 16, v244\n"
    "v_and_b32 v4, 0xffff0000, v244\n"
    "v_lshlrev_b32 v18, 16, v40\n"
    "v_and_b32 v19, 0xffff0000, v40\n"
    "v_add_f32 v18, v18, v3\n"
    "v_add_f32 v19, v19, v4\n"
    "v_lshlrev_b32 v52, 16, v44\n"
    "v_and_b32 v53, 0xffff0000, v44\n"
    "v_add_f32 v52, v52, v3\n"
    "v_add_f32 v53, v53, v4\n"
    "v_lshlrev_b32 v3, 16, v245\n"
    "v_and_b32 v4, 0xffff0000, v245\n"
    "v_lshlrev_b32 v20, 16, v41\n"
    "v_and_b32 v21, 0xffff0000, v41\n"
    "v_add_f32 v20, v20, v3\n"
    "v_add_f32 v21, v21, v4\n"
    "v_lshlrev_b32 v54, 16, v45\n"
    "v_and_b32 v55, 0xffff0000, v45\n"
    "v_add_f32 v54, v54, v3\n"
    "v_add_f32 v55, v55, v4\n"
    "v_lshlrev_b32 v3, 16, v246\n"
    "v_and_b32 v4, 0xffff0000, v246\n"
    "v_lshlrev_b32 v22, 16, v42\n"
    "v_and_b32 v23, 0xffff0000, v42\n"
    "v_add_f32 v22, v22, v3\n"
    "v_add_f32 v23, v23, v4\n"
    "v_lshlrev_b32 v56, 16, v46\n"
    "v_and_b32 v57, 0xffff0000, v46\n"
    "v_add_f32 v56, v56, v3\n"
    "v_add_f32 v57, v57, v4\n"
    "v_lshlrev_b32 v3, 16, v247\n"
    "v_and_b32 v4, 0xffff0000, v247\n"
    "v_lshlrev_b32 v24, 16, v43\n"
    "v_and_b32 v25, 0xffff0000, v43\n"
    "v_add_f32 v24, v24, v3\n"
    "v_add_f32 v25, v25, v4\n"
    "v_lshlrev_b32 v58, 16, v47\n"
    "v_and_b32 v59, 0xffff0000, v47\n"
    "v_add_f32 v58, v58, v3\n"
    "v_add_f32 v59, v59, v4\n"
    "v_and_b32 v3, 7, v32\n"
    "v_lshlrev_b32 v4, 1, v16\n"
    "v_xor_b32 v3, v3, v4\n"
    "v_lshlrev_b32 v3, 4, v3\n"
    "v_lshl_add_u32 v3, v32, 8, v3\n"
    "v_add_u32 v10, 0x18000, v3\n"
    "v_xor_b32 v11, 16, v10\n"
    "v_and_b32 v3, 3, v1\n"
    "v_bfe_u32 v4, v1, 2, 1\n"
    "v_lshlrev_b32 v5, 5, v3\n"
    "v_lshl_add_u32 v5, v4, 4, v5\n"
    "v_lshrrev_b32 v4, 3, v1\n"
    "v_lshl_add_u32 v5, v4, 7, v5\n"
    "s_mul_i32 s74, s69, 0x820\n"
    "s_add_u32 s74, s74, 0x20200\n"
    "v_add_u32 v14, s74, v5\n"
    "v_mul_u32_u24 v3, 0x410, v16\n"
    "v_lshl_add_u32 v3, v32, 2, v3\n"
    "v_add_u32 v15, 0x20200, v3\n"
    "v_lshlrev_b32 v13, 7, v50\n"
    "s_lshl_b32 s74, s79, 5\n"
    "s_add_u32 s74, s74, s93\n"
    "s_mul_i32 s74, s74, 0x2000\n"
    "s_lshr_b32 s75, s89, 1\n"
    "s_add_u32 s74, s74, s75\n"
    "s_add_u32 s87, s74, 0x250000\n"
    "s_lshr_b32 s74, s93, 4\n"
    "s_lshl_b32 s74, s74, 3\n"
    "s_add_u32 s74, s74, s79\n"
    "s_lshl_b32 s74, s74, 2\n"
    "s_add_u32 s74, s74, s69\n"
    "s_lshl_b32 s74, s74, 11\n"
    "s_add_u32 s88, s74, 0x50000\n"
    "s_mul_i32 s74, s80, 7\n"
    "s_add_u32 s74, s74, s79\n"
    "s_lshl_b32 s74, s74, 1\n"
    "s_add_u32 s74, s74, s81\n"
    "s_lshl_b32 s74, s74, 2\n"
    "s_add_u32 s74, s74, s69\n"
    "s_lshl_b32 s75, s74, 7\n"
    "s_add_u32 s83, s75, 0x70000\n"
    "s_lshl_b32 s74, s74, 11\n"
    "s_add_u32 s73, s74, 0x410000\n"
    "s_xor_b32 s74, s74, 0x2000\n"
    "s_add_u32 s72, s74, 0x410000\n"
    "s_waitcnt vmcnt(0)\n"
    "s_mov_b64 exec, 1\n"
    "buffer_store_dwordx2 v[8:9], off, s[52:55], s85 sc1\n"
    "s_mov_b64 exec, -1\n"
    "s_barrier\n"
    "s_setprio 3\n"
    "s_barrier\n"
    "ds_read_b128 v[60:63], v10\n"
    "ds_read_b128 v[64:67], v11\n"
    "ds_read_b128 v[68:71], v10 offset:8192\n"
    "ds_read_b128 v[72:75], v11 offset:8192\n"
    "ds_read_b128 v[76:79], v10 offset:42496\n"
    "ds_read_b128 v[80:83], v11 offset:42496\n"
    "ds_read_b128 v[84:87], v10 offset:50688\n"
    "ds_read_b128 v[88:91], v11 offset:50688\n"
    "buffer_load_dwordx2 v[30:31], v13, s[52:55], s88 offen sc1\n"
    "s_waitcnt lgkmcnt(5)\n"
    "v_add_f32 v60, v60, v68\n"
    "v_add_f32 v61, v61, v69\n"
    "v_add_f32 v62, v62, v70\n"
    "v_add_f32 v63, v63, v71\n"
    "s_waitcnt lgkmcnt(4)\n"
    "v_add_f32 v64, v64, v72\n"
    "v_add_f32 v65, v65, v73\n"
    "v_add_f32 v66, v66, v74\n"
    "v_add_f32 v67, v67, v75\n"
    "v_cvt_pk_bf16_f32 v100, v60, v61\n"
    "v_cvt_pk_bf16_f32 v101, v62, v63\n"
    "v_cvt_pk_bf16_f32 v102, v64, v65\n"
    "v_cvt_pk_bf16_f32 v103, v66, v67\n"
    "buffer_store_dwordx4 v[100:103], v2, s[52:55], s72 offen sc1\n"
    "s_waitcnt lgkmcnt(1)\n"
    "v_add_f32 v76, v76, v84\n"
    "v_add_f32 v77, v77, v85\n"
    "v_add_f32 v78, v78, v86\n"
    "v_add_f32 v79, v79, v87\n"
    "s_waitcnt lgkmcnt(0)\n"
    "v_add_f32 v80, v80, v88\n"
    "v_add_f32 v81, v81, v89\n"
    "v_add_f32 v82, v82, v90\n"
    "v_add_f32 v83, v83, v91\n"
    "v_cvt_pk_bf16_f32 v104, v76, v77\n"
    "v_cvt_pk_bf16_f32 v105, v78, v79\n"
    "v_cvt_pk_bf16_f32 v106, v80, v81\n"
    "v_cvt_pk_bf16_f32 v107, v82, v83\n"
    "buffer_store_dwordx4 v[104:107], v2, s[52:55], s72 offen offset:1024 sc1\n"
    "v_subrev_u32 v10, 0x10000, v10\n"
    "v_subrev_u32 v11, 0x10000, v11\n"
    "s_xor_b32 s74, s83, 0x200\n"
    "s_waitcnt vmcnt(2)\n"
    "v_cmp_ne_u64 vcc, s[76:77], v[30:31]\n"
    "s_nop 0\n"
    "s_cbranch_vccnz .Lslowy%=\n"
    ".Loky%=:\n"
    "buffer_load_dwordx4 v[26:29], v2, s[52:55], s87 offen sc1\n"
    "buffer_load_dwordx4 v[36:39], v2, s[52:55], s87 offen offset:1024 sc1\n"
    "s_waitcnt vmcnt(2)\n"
    "s_mov_b64 exec, 1\n"
    "buffer_store_dwordx2 v[8:9], off, s[52:55], s74 sc1\n"
    "s_mov_b64 exec, -1\n"
    "buffer_load_dwordx2 v[30:31], off, s[52:55], s83 sc1\n"
    "s_waitcnt vmcnt(3)\n"
    "ds_write_b128 v14, v[26:29]\n"
    "s_waitcnt vmcnt(2)\n"
    "ds_write_b128 v14, v[36:39] offset:1040\n"
    "s_mov_b64 exec, 1\n"
    "ds_write_b32 v96, v98 offset:48\n"
    "s_mov_b64 exec, -1\n"
    "ds_read_b32 v3, v12 offset:48\n"
    "s_waitcnt lgkmcnt(0)\n"
    "v_cmp_eq_u32 vcc, 0, v3\n"
    "s_nop 0\n"
    "s_cbranch_vccnz .Lslowu%=\n"
    ".Loku%=:\n"
    "ds_read_b32 v60, v15\n"
    "ds_read_b32 v61, v15 offset:128\n"
    "ds_read_b32 v62, v15 offset:256\n"
    "ds_read_b32 v63, v15 offset:384\n"
    "ds_read_b32 v64, v15 offset:512\n"
    "ds_read_b32 v65, v15 offset:640\n"
    "ds_read_b32 v66, v15 offset:768\n"
    "ds_read_b32 v67, v15 offset:896\n"
    "s_waitcnt lgkmcnt(7)\n"
    "v_lshlrev_b32 v3, 16, v60\n"
    "v_and_b32 v4, 0xffff0000, v60\n"
    "v_add_f32 v18, v18, v3\n"
    "v_add_f32 v52, v52, v4\n"
    "s_waitcnt lgkmcnt(6)\n"
    "v_lshlrev_b32 v3, 16, v61\n"
    "v_and_b32 v4, 0xffff0000, v61\n"
    "v_add_f32 v19, v19, v3\n"
    "v_add_f32 v53, v53, v4\n"
    "s_waitcnt lgkmcnt(5)\n"
    "v_lshlrev_b32 v3, 16, v62\n"
    "v_and_b32 v4, 0xffff0000, v62\n"
    "v_add_f32 v20, v20, v3\n"
    "v_add_f32 v54, v54, v4\n"
    "s_waitcnt lgkmcnt(4)\n"
    "v_lshlrev_b32 v3, 16, v63\n"
    "v_and_b32 v4, 0xffff0000, v63\n"
    "v_add_f32 v21, v21, v3\n"
    "v_add_f32 v55, v55, v4\n"
    "s_waitcnt lgkmcnt(3)\n"
    "v_lshlrev_b32 v3, 16, v64\n"
    "v_and_b32 v4, 0xffff0000, v64\n"
    "v_add_f32 v22, v22, v3\n"
    "v_add_f32 v56, v56, v4\n"
    "s_waitcnt lgkmcnt(2)\n"
    "v_lshlrev_b32 v3, 16, v65\n"
    "v_and_b32 v4, 0xffff0000, v65\n"
    "v_add_f32 v23, v23, v3\n"
    "v_add_f32 v57, v57, v4\n"
    "s_waitcnt lgkmcnt(1)\n"
    "v_lshlrev_b32 v3, 16, v66\n"
    "v_and_b32 v4, 0xffff0000, v66\n"
    "v_add_f32 v24, v24, v3\n"
    "v_add_f32 v58, v58, v4\n"
    "s_waitcnt lgkmcnt(0)\n"
    "v_lshlrev_b32 v3, 16, v67\n"
    "v_and_b32 v4, 0xffff0000, v67\n"
    "v_add_f32 v25, v25, v3\n"
    "v_add_f32 v59, v59, v4\n"
    "s_waitcnt vmcnt(0)\n"
    "v_cmp_ne_u64 vcc, s[76:77], v[30:31]\n"
    "s_nop 0\n"
    "s_cbranch_vccnz .Lslowp%=\n"
    ".Lokp%=:\n"
    "buffer_load_dwordx4 v[92:95], v2, s[52:55], s73 offen sc1\n"
    "buffer_load_dwordx4 v[108:111], v2, s[52:55], s73 offen offset:1024 sc1\n"
    "s_barrier\n"
    "ds_read_b128 v[60:63], v10\n"
    "ds_read_b128 v[64:67], v11\n"
    "ds_read_b128 v[68:71], v10 offset:8192\n"
    "ds_read_b128 v[72:75], v11 offset:8192\n"
    "ds_read_b128 v[76:79], v10 offset:16384\n"
    "ds_read_b128 v[80:83], v11 offset:16384\n"
    "ds_read_b128 v[84:87], v10 offset:24576\n"
    "ds_read_b128 v[88:91], v11 offset:24576\n"
    "s_waitcnt lgkmcnt(5)\n"
    "v_add_f32 v60, v60, v68\n"
    "v_fmac_f32 v18, v60, v34\n"
    "v_add_f32 v61, v61, v69\n"
    "v_fmac_f32 v19, v61, v34\n"
    "v_add_f32 v62, v62, v70\n"
    "v_fmac_f32 v20, v62, v34\n"
    "v_add_f32 v63, v63, v71\n"
    "v_fmac_f32 v21, v63, v34\n"
    "s_waitcnt lgkmcnt(4)\n"
    "v_add_f32 v64, v64, v72\n"
    "v_fmac_f32 v22, v64, v34\n"
    "v_add_f32 v65, v65, v73\n"
    "v_fmac_f32 v23, v65, v34\n"
    "v_add_f32 v66, v66, v74\n"
    "v_fmac_f32 v24, v66, v34\n"
    "v_add_f32 v67, v67, v75\n"
    "v_fmac_f32 v25, v67, v34\n"
    "s_waitcnt lgkmcnt(1)\n"
    "v_add_f32 v76, v76, v84\n"
    "v_fmac_f32 v52, v76, v35\n"
    "v_add_f32 v77, v77, v85\n"
    "v_fmac_f32 v53, v77, v35\n"
    "v_add_f32 v78, v78, v86\n"
    "v_fmac_f32 v54, v78, v35\n"
    "v_add_f32 v79, v79, v87\n"
    "v_fmac_f32 v55, v79, v35\n"
    "s_waitcnt lgkmcnt(0)\n"
    "v_add_f32 v80, v80, v88\n"
    "v_fmac_f32 v56, v80, v35\n"
    "v_add_f32 v81, v81, v89\n"
    "v_fmac_f32 v57, v81, v35\n"
    "v_add_f32 v82, v82, v90\n"
    "v_fmac_f32 v58, v82, v35\n"
    "v_add_f32 v83, v83, v91\n"
    "v_fmac_f32 v59, v83, v35\n"
    "s_waitcnt vmcnt(1)\n"
    "v_lshlrev_b32 v3, 16, v92\n"
    "v_and_b32 v4, 0xffff0000, v92\n"
    "v_fmac_f32 v18, v3, v34\n"
    "v_fmac_f32 v19, v4, v34\n"
    "v_lshlrev_b32 v3, 16, v93\n"
    "v_and_b32 v4, 0xffff0000, v93\n"
    "v_fmac_f32 v20, v3, v34\n"
    "v_fmac_f32 v21, v4, v34\n"
    "v_lshlrev_b32 v3, 16, v94\n"
    "v_and_b32 v4, 0xffff0000, v94\n"
    "v_fmac_f32 v22, v3, v34\n"
    "v_fmac_f32 v23, v4, v34\n"
    "v_lshlrev_b32 v3, 16, v95\n"
    "v_and_b32 v4, 0xffff0000, v95\n"
    "v_fmac_f32 v24, v3, v34\n"
    "v_fmac_f32 v25, v4, v34\n"
    "v_cvt_pk_bf16_f32 v40, v18, v19\n"
    "v_cvt_pk_bf16_f32 v41, v20, v21\n"
    "v_cvt_pk_bf16_f32 v42, v22, v23\n"
    "v_cvt_pk_bf16_f32 v43, v24, v25\n"
    "buffer_store_dwordx4 v[40:43], v33, s[56:59], s71 offen sc1\n"
    "s_waitcnt vmcnt(1)\n"
    "v_lshlrev_b32 v3, 16, v108\n"
    "v_and_b32 v4, 0xffff0000, v108\n"
    "v_fmac_f32 v52, v3, v35\n"
    "v_fmac_f32 v53, v4, v35\n"
    "v_lshlrev_b32 v3, 16, v109\n"
    "v_and_b32 v4, 0xffff0000, v109\n"
    "v_fmac_f32 v54, v3, v35\n"
    "v_fmac_f32 v55, v4, v35\n"
    "v_lshlrev_b32 v3, 16, v110\n"
    "v_and_b32 v4, 0xffff0000, v110\n"
    "v_fmac_f32 v56, v3, v35\n"
    "v_fmac_f32 v57, v4, v35\n"
    "v_lshlrev_b32 v3, 16, v111\n"
    "v_and_b32 v4, 0xffff0000, v111\n"
    "v_fmac_f32 v58, v3, v35\n"
    "v_fmac_f32 v59, v4, v35\n"
    "v_cvt_pk_bf16_f32 v44, v52, v53\n"
    "v_cvt_pk_bf16_f32 v45, v54, v55\n"
    "v_cvt_pk_bf16_f32 v46, v56, v57\n"
    "v_cvt_pk_bf16_f32 v47, v58, v59\n"
    "buffer_store_dwordx4 v[44:47], v33, s[56:59], s71 offen offset:896 sc1\n"
    "s_endpgm\n"
    ".Lce1%=:\n"
    "v_mfma_f32_16x16x32_bf16 v[52:55], v[132:135], v[196:199], v[52:55]\n"
    "v_mfma_f32_16x16x32_bf16 v[56:59], v[132:135], v[200:203], v[56:59]\n"
    "v_mfma_f32_16x16x32_bf16 v[56:59], v[136:139], v[196:199], v[56:59]\n"
    "v_mfma_f32_16x16x32_bf16 v[60:63], v[136:139], v[200:203], v[60:63]\n"
    "v_mfma_f32_16x16x32_bf16 v[60:63], v[132:135], v[204:207], v[60:63]\n"
    "v_mfma_f32_16x16x32_bf16 v[64:67], v[136:139], v[204:207], v[64:67]\n"
    "v_mfma_f32_16x16x32_bf16 v[60:63], v[140:143], v[196:199], v[60:63]\n"
    "v_mfma_f32_16x16x32_bf16 v[64:67], v[140:143], v[200:203], v[64:67]\n"
    "s_waitcnt lgkmcnt(12)\n"
    "v_mfma_f32_16x16x32_bf16 v[64:67], v[132:135], v[208:211], v[64:67]\n"
    "v_mfma_f32_16x16x32_bf16 v[64:67], v[144:147], v[196:199], v[64:67]\n"
    "v_mfma_f32_16x16x32_bf16 v[68:71], v[140:143], v[204:207], v[68:71]\n"
    "v_mfma_f32_16x16x32_bf16 v[68:71], v[136:139], v[208:211], v[68:71]\n"
    "v_mfma_f32_16x16x32_bf16 v[72:75], v[140:143], v[208:211], v[72:75]\n"
    "ds_write_b128 v14, v[52:55]\n"
    "v_mfma_f32_16x16x32_bf16 v[68:71], v[144:147], v[200:203], v[68:71]\n"
    "ds_write_b128 v14, v[56:59] offset:1024\n"
    "v_mfma_f32_16x16x32_bf16 v[72:75], v[144:147], v[204:207], v[72:75]\n"
    "v_mfma_f32_16x16x32_bf16 v[76:79], v[144:147], v[208:211], v[76:79]\n"
    "ds_write_b128 v14, v[60:63] offset:2048\n"
    "s_waitcnt lgkmcnt(12)\n"
    "v_mfma_f32_16x16x32_bf16 v[68:71], v[132:135], v[212:215], v[68:71]\n"
    "ds_write_b128 v14, v[64:67] offset:3072\n"
    "v_mfma_f32_16x16x32_bf16 v[72:75], v[136:139], v[212:215], v[72:75]\n"
    "v_mfma_f32_16x16x32_bf16 v[76:79], v[140:143], v[212:215], v[76:79]\n"
    "s_mov_b64 exec, 1\n"
    "ds_write_b32 v96, v98 offset:32\n"
    "s_mov_b64 exec, -1\n"
    "v_mfma_f32_16x16x32_bf16 v[80:83], v[144:147], v[212:215], v[80:83]\n"
    "v_mfma_f32_16x16x32_bf16 v[68:71], v[148:151], v[196:199], v[68:71]\n"
    "v_mfma_f32_16x16x32_bf16 v[72:75], v[148:151], v[200:203], v[72:75]\n"
    "v_mfma_f32_16x16x32_bf16 v[76:79], v[148:151], v[204:207], v[76:79]\n"
    "v_mfma_f32_16x16x32_bf16 v[80:83], v[148:151], v[208:211], v[80:83]\n"
    "s_waitcnt lgkmcnt(11)\n"
    "v_mfma_f32_16x16x32_bf16 v[72:75], v[132:135], v[216:219], v[72:75]\n"
    "v_mfma_f32_16x16x32_bf16 v[76:79], v[136:139], v[216:219], v[76:79]\n"
    "v_mfma_f32_16x16x32_bf16 v[80:83], v[140:143], v[216:219], v[80:83]\n"
    "v_mfma_f32_16x16x32_bf16 v[72:75], v[152:155], v[196:199], v[72:75]\n"
    "v_mfma_f32_16x16x32_bf16 v[76:79], v[152:155], v[200:203], v[76:79]\n"
    "v_mfma_f32_16x16x32_bf16 v[80:83], v[152:155], v[204:207], v[80:83]\n"
    "s_waitcnt lgkmcnt(8)\n"
    "v_mfma_f32_16x16x32_bf16 v[76:79], v[132:135], v[220:223], v[76:79]\n"
    "v_mfma_f32_16x16x32_bf16 v[80:83], v[136:139], v[220:223], v[80:83]\n"
    "v_mfma_f32_16x16x32_bf16 v[76:79], v[156:159], v[196:199], v[76:79]\n"
    "v_mfma_f32_16x16x32_bf16 v[80:83], v[156:159], v[200:203], v[80:83]\n"
    "s_waitcnt lgkmcnt(5)\n"
    "v_mfma_f32_16x16x32_bf16 v[80:83], v[132:135], v[224:227], v[80:83]\n"
    "v_mfma_f32_16x16x32_bf16 v[80:83], v[160:163], v[196:199], v[80:83]\n"
    "s_setprio 1\n"
    "ds_read_b32 v3, v97 offset:32\n"
    "s_waitcnt lgkmcnt(0)\n"
    "v_cmp_eq_u32 vcc, 0, v3\n"
    "s_nop 0\n"
    "s_cbranch_vccnz .Lslowt31%=\n"
    ".Lokt31%=:\n"
    "ds_read_b128 v[164:167], v15\n"
    "ds_read_b128 v[168:171], v15 offset:1024\n"
    "ds_read_b128 v[172:175], v15 offset:2048\n"
    "ds_read_b128 v[176:179], v15 offset:3072\n"
    "s_waitcnt lgkmcnt(3)\n"
    "v_add_f32 v164, v164, v68\n"
    "v_add_f32 v165, v165, v69\n"
    "v_add_f32 v166, v166, v70\n"
    "v_add_f32 v167, v167, v71\n"
    "v_cvt_pk_bf16_f32 v100, v164, v165\n"
    "v_cvt_pk_bf16_f32 v101, v166, v167\n"
    "s_waitcnt lgkmcnt(2)\n"
    "v_add_f32 v168, v168, v72\n"
    "v_add_f32 v169, v169, v73\n"
    "v_add_f32 v170, v170, v74\n"
    "v_add_f32 v171, v171, v75\n"
    "v_cvt_pk_bf16_f32 v102, v168, v169\n"
    "v_cvt_pk_bf16_f32 v103, v170, v171\n"
    "s_nop 0\n"
    "v_permlane16_swap_b32 v100, v102\n"
    "v_permlane16_swap_b32 v101, v103\n"
    "buffer_store_dwordx4 v[100:103], v10, s[52:55], s87 offen sc1\n"
    "s_add_u32 s87, s87, 0x10000\n"
    "s_waitcnt lgkmcnt(1)\n"
    "v_add_f32 v172, v172, v76\n"
    "v_add_f32 v173, v173, v77\n"
    "v_add_f32 v174, v174, v78\n"
    "v_add_f32 v175, v175, v79\n"
    "v_cvt_pk_bf16_f32 v104, v172, v173\n"
    "v_cvt_pk_bf16_f32 v105, v174, v175\n"
    "s_waitcnt lgkmcnt(0)\n"
    "v_add_f32 v176, v176, v80\n"
    "v_add_f32 v177, v177, v81\n"
    "v_add_f32 v178, v178, v82\n"
    "v_add_f32 v179, v179, v83\n"
    "v_cvt_pk_bf16_f32 v106, v176, v177\n"
    "v_cvt_pk_bf16_f32 v107, v178, v179\n"
    "s_nop 1\n"
    "s_branch .Lcej%=\n"
    ".Lslowe%=:\n"
    "s_mov_b32 s78, 0x10000\n"
    ".Lloope%=:\n"
    "s_nop 3\n"
    "s_sleep 2\n"
    "buffer_load_dwordx2 v[200:201], v38, s[52:55], s72 offen sc1\n"
    "buffer_load_dwordx2 v[202:203], v38, s[52:55], s72 offen offset:128 sc1\n"
    "buffer_load_dwordx2 v[204:205], v38, s[52:55], s73 offen sc1\n"
    "buffer_load_dwordx2 v[206:207], v38, s[52:55], s73 offen offset:128 sc1\n"
    "s_waitcnt vmcnt(0)\n"
    "v_xor_b32 v3, s76, v201\n"
    "v_xor_b32 v4, s76, v203\n"
    "v_xor_b32 v5, s76, v205\n"
    "v_or3_b32 v3, v3, v4, v5\n"
    "v_xor_b32 v4, s76, v207\n"
    "v_or_b32 v3, v3, v4\n"
    "v_cmp_ne_u32 vcc, 0, v3\n"
    "s_nop 1\n"
    "s_cbranch_vccz .Loke%=\n"
    "s_sub_u32 s78, s78, 1\n"
    "s_cmp_lg_u32 s78, 0\n"
    "s_cbranch_scc1 .Lloope%=\n"
    "s_branch .Loke%=\n"
    ".Lslows6%=:\n"
    "s_mov_b32 s78, 0x10000\n"
    ".Lloops6%=:\n"
    "s_nop 3\n"
    "ds_read_b32 v5, v6 offset:112\n"
    "s_waitcnt lgkmcnt(0)\n"
    "v_cmp_eq_u32 vcc, 0, v5\n"
    "s_nop 1\n"
    "s_cbranch_vccz .Loks6%=\n"
    "s_sub_u32 s78, s78, 1\n"
    "s_cmp_lg_u32 s78, 0\n"
    "s_cbranch_scc1 .Lloops6%=\n"
    "s_branch .Loks6%=\n"
    ".Lslowh%=:\n"
    "s_mov_b32 s78, 0x10000\n"
    ".Llooph%=:\n"
    "s_nop 3\n"
    "s_sleep 2\n"
    "buffer_load_dwordx2 v[222:223], v39, s[52:55], s70 offen sc1\n"
    "s_waitcnt vmcnt(0)\n"
    "v_cmp_ne_u64 vcc, s[76:77], v[222:223]\n"
    "s_nop 1\n"
    "s_cbranch_vccz .Lokh%=\n"
    "s_sub_u32 s78, s78, 1\n"
    "s_cmp_lg_u32 s78, 0\n"
    "s_cbranch_scc1 .Llooph%=\n"
    "s_branch .Lokh%=\n"
    ".Lslows1%=:\n"
    "s_mov_b32 s78, 0x10000\n"
    ".Lloops1%=:\n"
    "s_nop 3\n"
    "ds_read_b32 v3, v220 offset:16\n"
    "s_waitcnt lgkmcnt(0)\n"
    "v_cmp_eq_u32 vcc, 0, v3\n"
    "s_nop 1\n"
    "s_cbranch_vccz .Loks1%=\n"
    "s_sub_u32 s78, s78, 1\n"
    "s_cmp_lg_u32 s78, 0\n"
    "s_cbranch_scc1 .Lloops1%=\n"
    "s_branch .Loks1%=\n"
    ".Lslows2%=:\n"
    "s_mov_b32 s78, 0x10000\n"
    ".Lloops2%=:\n"
    "s_nop 3\n"
    "ds_read_b32 v3, v220\n"
    "s_waitcnt lgkmcnt(0)\n"
    "v_cmp_eq_u32 vcc, 0, v3\n"
    "s_nop 1\n"
    "s_cbranch_vccz .Loks2%=\n"
    "s_sub_u32 s78, s78, 1\n"
    "s_cmp_lg_u32 s78, 0\n"
    "s_cbranch_scc1 .Lloops2%=\n"
    "s_branch .Loks2%=\n"
    ".Lslowh1%=:\n"
    "s_mov_b32 s78, 0x10000\n"
    ".Llooph1%=:\n"
    "s_nop 3\n"
    "s_sleep 2\n"
    "buffer_load_dwordx2 v[222:223], v39, s[52:55], s70 offen sc1\n"
    "s_waitcnt vmcnt(0)\n"
    "v_cmp_ne_u64 vcc, s[76:77], v[222:223]\n"
    "s_nop 1\n"
    "s_cbranch_vccz .Lokh1%=\n"
    "s_sub_u32 s78, s78, 1\n"
    "s_cmp_lg_u32 s78, 0\n"
    "s_cbranch_scc1 .Llooph1%=\n"
    "s_branch .Lokh1%=\n"
    ".Lslowx%=:\n"
    "s_mov_b32 s78, 0x10000\n"
    ".Lloopx%=:\n"
    "s_nop 3\n"
    "s_sleep 6\n"
    "buffer_load_dwordx2 v[30:31], v13, s[52:55], 0 offen sc1\n"
    "s_waitcnt vmcnt(0)\n"
    "v_cmp_ne_u64 vcc, s[76:77], v[30:31]\n"
    "s_nop 1\n"
    "s_cbranch_vccz .Lokx%=\n"
    "s_sub_u32 s78, s78, 1\n"
    "s_cmp_lg_u32 s78, 0\n"
    "s_cbranch_scc1 .Lloopx%=\n"
    "s_branch .Lokx%=\n"
    ".Lslowt0%=:\n"
    "s_mov_b32 s78, 0x10000\n"
    ".Lloopt0%=:\n"
    "s_nop 3\n"
    "ds_read_b32 v3, v12 offset:112\n"
    "s_waitcnt lgkmcnt(0)\n"
    "v_cmp_eq_u32 vcc, 0, v3\n"
    "s_nop 1\n"
    "s_cbranch_vccz .Lokt0%=\n"
    "s_sub_u32 s78, s78, 1\n"
    "s_cmp_lg_u32 s78, 0\n"
    "s_cbranch_scc1 .Lloopt0%=\n"
    "s_branch .Lokt0%=\n"
    ".Lslowt2%=:\n"
    "s_mov_b32 s78, 0x10000\n"
    ".Lloopt2%=:\n"
    "s_nop 3\n"
    "ds_read_b32 v3, v97 offset:80\n"
    "s_waitcnt lgkmcnt(0)\n"
    "v_cmp_eq_u32 vcc, 0, v3\n"
    "s_nop 1\n"
    "s_cbranch_vccz .Lokt2%=\n"
    "s_sub_u32 s78, s78, 1\n"
    "s_cmp_lg_u32 s78, 0\n"
    "s_cbranch_scc1 .Lloopt2%=\n"
    "s_branch .Lokt2%=\n"
    ".Lslowt31%=:\n"
    "s_mov_b32 s78, 0x10000\n"
    ".Lloopt31%=:\n"
    "s_nop 3\n"
    "ds_read_b32 v3, v97 offset:32\n"
    "s_waitcnt lgkmcnt(0)\n"
    "v_cmp_eq_u32 vcc, 0, v3\n"
    "s_nop 1\n"
    "s_cbranch_vccz .Lokt31%=\n"
    "s_sub_u32 s78, s78, 1\n"
    "s_cmp_lg_u32 s78, 0\n"
    "s_cbranch_scc1 .Lloopt31%=\n"
    "s_branch .Lokt31%=\n"
    ".Lslowt30%=:\n"
    "s_mov_b32 s78, 0x10000\n"
    ".Lloopt30%=:\n"
    "s_nop 3\n"
    "ds_read_b32 v3, v97 offset:32\n"
    "s_waitcnt lgkmcnt(0)\n"
    "v_cmp_eq_u32 vcc, 0, v3\n"
    "s_nop 1\n"
    "s_cbranch_vccz .Lokt30%=\n"
    "s_sub_u32 s78, s78, 1\n"
    "s_cmp_lg_u32 s78, 0\n"
    "s_cbranch_scc1 .Lloopt30%=\n"
    "s_branch .Lokt30%=\n"
    ".Lslowy%=:\n"
    "s_mov_b32 s78, 0x10000\n"
    ".Lloopy%=:\n"
    "s_nop 3\n"
    "s_sleep 2\n"
    "buffer_load_dwordx2 v[30:31], v13, s[52:55], s88 offen sc1\n"
    "s_waitcnt vmcnt(0)\n"
    "v_cmp_ne_u64 vcc, s[76:77], v[30:31]\n"
    "s_nop 1\n"
    "s_cbranch_vccz .Loky%=\n"
    "s_sub_u32 s78, s78, 1\n"
    "s_cmp_lg_u32 s78, 0\n"
    "s_cbranch_scc1 .Lloopy%=\n"
    "s_branch .Loky%=\n"
    ".Lslowu%=:\n"
    "s_mov_b32 s78, 0x10000\n"
    ".Lloopu%=:\n"
    "s_nop 3\n"
    "ds_read_b32 v3, v12 offset:48\n"
    "s_waitcnt lgkmcnt(0)\n"
    "v_cmp_eq_u32 vcc, 0, v3\n"
    "s_nop 1\n"
    "s_cbranch_vccz .Loku%=\n"
    "s_sub_u32 s78, s78, 1\n"
    "s_cmp_lg_u32 s78, 0\n"
    "s_cbranch_scc1 .Lloopu%=\n"
    "s_branch .Loku%=\n"
    ".Lslowp%=:\n"
    "s_mov_b32 s78, 0x10000\n"
    ".Lloopp%=:\n"
    "s_nop 3\n"
    "s_sleep 2\n"
    "buffer_load_dwordx2 v[30:31], off, s[52:55], s83 sc1\n"
    "s_waitcnt vmcnt(0)\n"
    "v_cmp_ne_u64 vcc, s[76:77], v[30:31]\n"
    "s_nop 1\n"
    "s_cbranch_vccz .Lokp%=\n"
    "s_sub_u32 s78, s78, 1\n"
    "s_cmp_lg_u32 s78, 0\n"
    "s_cbranch_scc1 .Lloopp%=\n"
    "s_branch .Lokp%=\n"
    ".Lslowep%=:\n"
    "s_mov_b32 s78, 0x10000\n"
    ".Lloopep%=:\n"
    "s_nop 3\n"
    "s_sleep 24\n"
    "s_mov_b32 s75, 0x1000\n"
    "buffer_load_dwordx2 v[72:73], v4, s[52:55], s75 offen sc1\n"
    "s_mov_b32 s74, 0x10000\n"
    "buffer_load_dwordx2 v[64:65], v3, s[52:55], s74 offen sc1\n"
    "s_mov_b32 s74, 0x20000\n"
    "buffer_load_dwordx2 v[66:67], v3, s[52:55], s74 offen sc1\n"
    "s_mov_b32 s74, 0x30000\n"
    "buffer_load_dwordx2 v[68:69], v3, s[52:55], s74 offen sc1\n"
    "s_mov_b32 s74, 0x40000\n"
    "buffer_load_dwordx2 v[70:71], v3, s[52:55], s74 offen sc1\n"
    "s_waitcnt vmcnt(0)\n"
    "v_mov_b32 v5, 0\n"
    "v_xor_b32 v64, s76, v64\n"
    "v_xor_b32 v65, s77, v65\n"
    "v_or3_b32 v5, v5, v64, v65\n"
    "v_xor_b32 v66, s76, v66\n"
    "v_xor_b32 v67, s77, v67\n"
    "v_or3_b32 v5, v5, v66, v67\n"
    "v_xor_b32 v68, s76, v68\n"
    "v_xor_b32 v69, s77, v69\n"
    "v_or3_b32 v5, v5, v68, v69\n"
    "v_xor_b32 v70, s76, v70\n"
    "v_xor_b32 v71, s77, v71\n"
    "v_or3_b32 v5, v5, v70, v71\n"
    "v_xor_b32 v72, s76, v72\n"
    "v_xor_b32 v73, s77, v73\n"
    "v_or3_b32 v5, v5, v72, v73\n"
    "v_cmp_ne_u32 vcc, 0, v5\n"
    "s_nop 1\n"
    "s_cbranch_vccz .Lokep%=\n"
    "s_sub_u32 s78, s78, 1\n"
    "s_cmp_lg_u32 s78, 0\n"
    "s_cbranch_scc1 .Lloopep%=\n"
    "s_branch .Lokep%=\n"
    ".Lhelper%=:\n"
    "v_lshrrev_b32 v3, 3, v1\n"
    "v_and_b32 v5, 7, v1\n"
    "v_mul_u32_u24 v10, 0x380, v3\n"
    "v_lshl_add_u32 v10, v5, 4, v10\n"
    "v_add_u32 v10, s81, v10\n"
    "v_mov_b32 v11, 0\n"
    "v_mov_b32 v40, 0\n"
    "v_mov_b32 v41, 0\n"
    "v_mov_b32 v42, 0\n"
    "s_waitcnt vmcnt(0)\n"
    "ds_read_b128 v[12:15], v10\n"
    "ds_read_b128 v[16:19], v10 offset:128\n"
    "ds_read_b128 v[20:23], v10 offset:256\n"
    "ds_read_b128 v[24:27], v10 offset:384\n"
    "ds_read_b128 v[28:31], v10 offset:512\n"
    "ds_read_b128 v[32:35], v10 offset:640\n"
    "ds_read_b128 v[36:39], v10 offset:768\n"
    "v_mov_b32 v44, 0x3f803f80\n"
    "s_waitcnt lgkmcnt(0)\n"
    "v_dot2c_f32_bf16 v11, v44, v12\n"
    "v_dot2c_f32_bf16 v41, v12, v12\n"
    "v_dot2c_f32_bf16 v40, v44, v13\n"
    "v_dot2c_f32_bf16 v42, v13, v13\n"
    "v_dot2c_f32_bf16 v11, v44, v14\n"
    "v_dot2c_f32_bf16 v41, v14, v14\n"
    "v_dot2c_f32_bf16 v40, v44, v15\n"
    "v_dot2c_f32_bf16 v42, v15, v15\n"
    "v_dot2c_f32_bf16 v11, v44, v16\n"
    "v_dot2c_f32_bf16 v41, v16, v16\n"
    "v_dot2c_f32_bf16 v40, v44, v17\n"
    "v_dot2c_f32_bf16 v42, v17, v17\n"
    "v_dot2c_f32_bf16 v11, v44, v18\n"
    "v_dot2c_f32_bf16 v41, v18, v18\n"
    "v_dot2c_f32_bf16 v40, v44, v19\n"
    "v_dot2c_f32_bf16 v42, v19, v19\n"
    "v_dot2c_f32_bf16 v11, v44, v20\n"
    "v_dot2c_f32_bf16 v41, v20, v20\n"
    "v_dot2c_f32_bf16 v40, v44, v21\n"
    "v_dot2c_f32_bf16 v42, v21, v21\n"
    "v_dot2c_f32_bf16 v11, v44, v22\n"
    "v_dot2c_f32_bf16 v41, v22, v22\n"
    "v_dot2c_f32_bf16 v40, v44, v23\n"
    "v_dot2c_f32_bf16 v42, v23, v23\n"
    "v_dot2c_f32_bf16 v11, v44, v24\n"
    "v_dot2c_f32_bf16 v41, v24, v24\n"
    "v_dot2c_f32_bf16 v40, v44, v25\n"
    "v_dot2c_f32_bf16 v42, v25, v25\n"
    "v_dot2c_f32_bf16 v11, v44, v26\n"
    "v_dot2c_f32_bf16 v41, v26, v26\n"
    "v_dot2c_f32_bf16 v40, v44, v27\n"
    "v_dot2c_f32_bf16 v42, v27, v27\n"
    "v_dot2c_f32_bf16 v11, v44, v28\n"
    "v_dot2c_f32_bf16 v41, v28, v28\n"
    "v_dot2c_f32_bf16 v40, v44, v29\n"
    "v_dot2c_f32_bf16 v42, v29, v29\n"
    "v_dot2c_f32_bf16 v11, v44, v30\n"
    "v_dot2c_f32_bf16 v41, v30, v30\n"
    "v_dot2c_f32_bf16 v40, v44, v31\n"
    "v_dot2c_f32_bf16 v42, v31, v31\n"
    "v_dot2c_f32_bf16 v11, v44, v32\n"
    "v_dot2c_f32_bf16 v41, v32, v32\n"
    "v_dot2c_f32_bf16 v40, v44, v33\n"
    "v_dot2c_f32_bf16 v42, v33, v33\n"
    "v_dot2c_f32_bf16 v11, v44, v34\n"
    "v_dot2c_f32_bf16 v41, v34, v34\n"
    "v_dot2c_f32_bf16 v40, v44, v35\n"
    "v_dot2c_f32_bf16 v42, v35, v35\n"
    "v_dot2c_f32_bf16 v11, v44, v36\n"
    "v_dot2c_f32_bf16 v41, v36, v36\n"
    "v_dot2c_f32_bf16 v40, v44, v37\n"
    "v_dot2c_f32_bf16 v42, v37, v37\n"
    "v_dot2c_f32_bf16 v11, v44, v38\n"
    "v_dot2c_f32_bf16 v41, v38, v38\n"
    "v_dot2c_f32_bf16 v40, v44, v39\n"
    "v_dot2c_f32_bf16 v42, v39, v39\n"
    "s_add_u32 s76, s76, 1\n"
    "s_addc_u32 s77, s77, 0\n"
    "v_mov_b32 v8, s76\n"
    "v_mov_b32 v9, s77\n"
    "v_add_f32 v11, v11, v40\n"
    "v_add_f32 v41, v41, v42\n"
    "s_nop 0\n"
    "v_add_f32_dpp v11, v11, v11 quad_perm:[1,0,3,2] row_mask:0xf bank_mask:0xf\n"
    "v_add_f32_dpp v41, v41, v41 quad_perm:[1,0,3,2] row_mask:0xf bank_mask:0xf\n"
    "s_nop 0\n"
    "v_add_f32_dpp v11, v11, v11 quad_perm:[2,3,0,1] row_mask:0xf bank_mask:0xf\n"
    "v_add_f32_dpp v41, v41, v41 quad_perm:[2,3,0,1] row_mask:0xf bank_mask:0xf\n"
    "s_nop 0\n"
    "v_add_f32_dpp v11, v11, v11 row_half_mirror row_mask:0xf bank_mask:0xf\n"
    "v_add_f32_dpp v41, v41, v41 row_half_mirror row_mask:0xf bank_mask:0xf\n"
    "v_mul_f32 v11, 0x3b124925, v11\n"
    "v_mul_f32 v41, 0x3b124925, v41\n"
    "v_fma_f32 v41, -v11, v11, v41\n"
    "v_max_f32 v41, 0, v41\n"
    "v_cvt_pk_bf16_f32 v41, v41, v41\n"
    "v_lshlrev_b32 v41, 16, v41\n"
    "v_add_f32 v41, 0x3727c5ac, v41\n"
    "v_cvt_pk_bf16_f32 v41, v41, v41\n"
    "v_lshlrev_b32 v41, 16, v41\n"
    "v_sqrt_f32 v41, v41\n"
    "v_sub_f32 v11, 0, v11\n"
    "v_cvt_pk_bf16_f32 v50, v11, v41\n"
    "v_mov_b32 v51, s76\n"
    "v_and_b32 v41, 0xffff0000, v50\n"
    "v_rcp_f32 v48, v41\n"
    "v_lshlrev_b32 v11, 16, v50\n"
    "v_lshrrev_b32 v4, 3, v0\n"
    "v_mul_f32 v49, v11, v48\n"
    "v_lshlrev_b32 v40, 3, v4\n"
    "v_and_b32 v6, 1, v4\n"
    "v_lshrrev_b32 v4, 1, v4\n"
    "v_lshl_add_u32 v4, v6, 5, v4\n"
    "v_lshlrev_b32 v4, 3, v4\n"
    "v_add_u32 v4, 0xeb00, v4\n"
    "s_mov_b32 exec_lo, 0x1010101\n"
    "s_mov_b32 exec_hi, 0x1010101\n"
    "ds_write_b64 v40, v[48:49] offset:57600\n"
    "ds_write_b64 v4, v[50:51]\n"
    "s_mov_b64 exec, -1\n"
    "v_mul_u32_u24 v28, 0x1c10, v5\n"
    "v_lshl_add_u32 v28, v3, 2, v28\n"
    "s_lshl_b32 s74, s68, 7\n"
    "v_add_u32 v28, s74, v28\n"
    "v_add_u32 v29, 0x700, v28\n"
    "v_add_u32 v30, 0xe00, v28\n"
    "v_add_u32 v31, 0x1500, v28\n"
    "v_mul_u32_u24 v10, 0x2000, v3\n"
    "v_lshl_add_u32 v10, v5, 4, v10\n"
    "v_lshlrev_b32 v4, 6, v5\n"
    "s_mul_i32 s74, s68, 0x40000\n"
    "s_lshl_b32 s75, s79, 8\n"
    "s_add_u32 s74, s74, s75\n"
    "s_add_u32 s82, s74, 0x90000\n"
    "v_lshlrev_b32 v6, 2, v0\n"
    "s_waitcnt lgkmcnt(0)\n"
    "s_barrier\n"
    "s_cmp_eq_u32 s68, 7\n"
    "s_cbranch_scc1 .Lcsum%=\n"
    ".Lcut2%=:\n"
    "ds_read_b128 v[12:15], v4 offset:57600\n"
    "ds_read_b128 v[16:19], v4 offset:57616\n"
    "ds_read_b128 v[20:23], v4 offset:57632\n"
    "ds_read_b128 v[24:27], v4 offset:57648\n"
    "ds_read2_b32 v[32:33], v28 offset0:0 offset1:224\n"
    "ds_read2_b32 v[34:35], v29 offset0:0 offset1:224\n"
    "ds_read2_b32 v[36:37], v30 offset0:0 offset1:224\n"
    "ds_read2_b32 v[38:39], v31 offset0:0 offset1:224\n"
    "ds_read2_b32 v[52:53], v28 offset0:8 offset1:232\n"
    "ds_read2_b32 v[54:55], v29 offset0:8 offset1:232\n"
    "ds_read2_b32 v[56:57], v30 offset0:8 offset1:232\n"
    "ds_read2_b32 v[58:59], v31 offset0:8 offset1:232\n"
    "s_waitcnt lgkmcnt(7)\n"
    "v_lshlrev_b32 v60, 16, v32\n"
    "v_fma_f32 v60, v60, v12, v13\n"
    "v_lshlrev_b32 v61, 16, v33\n"
    "v_fma_f32 v61, v61, v14, v15\n"
    "s_waitcnt lgkmcnt(6)\n"
    "v_lshlrev_b32 v62, 16, v34\n"
    "v_fma_f32 v62, v62, v16, v17\n"
    "v_lshlrev_b32 v63, 16, v35\n"
    "v_fma_f32 v63, v63, v18, v19\n"
    "s_waitcnt lgkmcnt(5)\n"
    "v_lshlrev_b32 v64, 16, v36\n"
    "v_fma_f32 v64, v64, v20, v21\n"
    "v_lshlrev_b32 v65, 16, v37\n"
    "v_fma_f32 v65, v65, v22, v23\n"
    "s_waitcnt lgkmcnt(4)\n"
    "v_lshlrev_b32 v66, 16, v38\n"
    "v_fma_f32 v66, v66, v24, v25\n"
    "v_lshlrev_b32 v67, 16, v39\n"
    "v_fma_f32 v67, v67, v26, v27\n"
    "v_cvt_pk_bf16_f32 v76, v60, v61\n"
    "v_cvt_pk_bf16_f32 v77, v62, v63\n"
    "v_cvt_pk_bf16_f32 v78, v64, v65\n"
    "v_cvt_pk_bf16_f32 v79, v66, v67\n"
    "buffer_store_dwordx4 v[76:79], v10, s[52:55], s82 offen sc1\n"
    "v_and_b32 v60, 0xffff0000, v32\n"
    "v_fma_f32 v60, v60, v12, v13\n"
    "v_and_b32 v61, 0xffff0000, v33\n"
    "v_fma_f32 v61, v61, v14, v15\n"
    "v_and_b32 v62, 0xffff0000, v34\n"
    "v_fma_f32 v62, v62, v16, v17\n"
    "v_and_b32 v63, 0xffff0000, v35\n"
    "v_fma_f32 v63, v63, v18, v19\n"
    "v_and_b32 v64, 0xffff0000, v36\n"
    "v_fma_f32 v64, v64, v20, v21\n"
    "v_and_b32 v65, 0xffff0000, v37\n"
    "v_fma_f32 v65, v65, v22, v23\n"
    "v_and_b32 v66, 0xffff0000, v38\n"
    "v_fma_f32 v66, v66, v24, v25\n"
    "v_and_b32 v67, 0xffff0000, v39\n"
    "v_fma_f32 v67, v67, v26, v27\n"
    "v_cvt_pk_bf16_f32 v80, v60, v61\n"
    "v_cvt_pk_bf16_f32 v81, v62, v63\n"
    "v_cvt_pk_bf16_f32 v82, v64, v65\n"
    "v_cvt_pk_bf16_f32 v83, v66, v67\n"
    "buffer_store_dwordx4 v[80:83], v10, s[52:55], s82 offen offset:128 sc1\n"
    "s_add_u32 s82, s82, 0x10000\n"
    "ds_read2_b32 v[32:33], v28 offset0:16 offset1:240\n"
    "ds_read2_b32 v[34:35], v29 offset0:16 offset1:240\n"
    "ds_read2_b32 v[36:37], v30 offset0:16 offset1:240\n"
    "ds_read2_b32 v[38:39], v31 offset0:16 offset1:240\n"
    "s_waitcnt lgkmcnt(7)\n"
    "v_lshlrev_b32 v60, 16, v52\n"
    "v_fma_f32 v60, v60, v12, v13\n"
    "v_lshlrev_b32 v61, 16, v53\n"
    "v_fma_f32 v61, v61, v14, v15\n"
    "s_waitcnt lgkmcnt(6)\n"
    "v_lshlrev_b32 v62, 16, v54\n"
    "v_fma_f32 v62, v62, v16, v17\n"
    "v_lshlrev_b32 v63, 16, v55\n"
    "v_fma_f32 v63, v63, v18, v19\n"
    "s_waitcnt lgkmcnt(5)\n"
    "v_lshlrev_b32 v64, 16, v56\n"
    "v_fma_f32 v64, v64, v20, v21\n"
    "v_lshlrev_b32 v65, 16, v57\n"
    "v_fma_f32 v65, v65, v22, v23\n"
    "s_waitcnt lgkmcnt(4)\n"
    "v_lshlrev_b32 v66, 16, v58\n"
    "v_fma_f32 v66, v66, v24, v25\n"
    "v_lshlrev_b32 v67, 16, v59\n"
    "v_fma_f32 v67, v67, v26, v27\n"
    "v_cvt_pk_bf16_f32 v84, v60, v61\n"
    "v_cvt_pk_bf16_f32 v85, v62, v63\n"
    "v_cvt_pk_bf16_f32 v86, v64, v65\n"
    "v_cvt_pk_bf16_f32 v87, v66, v67\n"
    "buffer_store_dwordx4 v[84:87], v10, s[52:55], s82 offen sc1\n"
    "v_and_b32 v60, 0xffff0000, v52\n"
    "v_fma_f32 v60, v60, v12, v13\n"
    "v_and_b32 v61, 0xffff0000, v53\n"
    "v_fma_f32 v61, v61, v14, v15\n"
    "v_and_b32 v62, 0xffff0000, v54\n"
    "v_fma_f32 v62, v62, v16, v17\n"
    "v_and_b32 v63, 0xffff0000, v55\n"
    "v_fma_f32 v63, v63, v18, v19\n"
    "v_and_b32 v64, 0xffff0000, v56\n"
    "v_fma_f32 v64, v64, v20, v21\n"
    "v_and_b32 v65, 0xffff0000, v57\n"
    "v_fma_f32 v65, v65, v22, v23\n"
    "v_and_b32 v66, 0xffff0000, v58\n"
    "v_fma_f32 v66, v66, v24, v25\n"
    "v_and_b32 v67, 0xffff0000, v59\n"
    "v_fma_f32 v67, v67, v26, v27\n"
    "v_cvt_pk_bf16_f32 v88, v60, v61\n"
    "v_cvt_pk_bf16_f32 v89, v62, v63\n"
    "v_cvt_pk_bf16_f32 v90, v64, v65\n"
    "v_cvt_pk_bf16_f32 v91, v66, v67\n"
    "buffer_store_dwordx4 v[88:91], v10, s[52:55], s82 offen offset:128 sc1\n"
    "s_add_u32 s82, s82, 0x10000\n"
    "ds_read2_b32 v[52:53], v28 offset0:24 offset1:248\n"
    "ds_read2_b32 v[54:55], v29 offset0:24 offset1:248\n"
    "ds_read2_b32 v[56:57], v30 offset0:24 offset1:248\n"
    "ds_read2_b32 v[58:59], v31 offset0:24 offset1:248\n"
    "s_waitcnt lgkmcnt(7)\n"
    "v_lshlrev_b32 v60, 16, v32\n"
    "v_fma_f32 v60, v60, v12, v13\n"
    "v_lshlrev_b32 v61, 16, v33\n"
    "v_fma_f32 v61, v61, v14, v15\n"
    "s_waitcnt lgkmcnt(6)\n"
    "v_lshlrev_b32 v62, 16, v34\n"
    "v_fma_f32 v62, v62, v16, v17\n"
    "v_lshlrev_b32 v63, 16, v35\n"
    "v_fma_f32 v63, v63, v18, v19\n"
    "s_waitcnt lgkmcnt(5)\n"
    "v_lshlrev_b32 v64, 16, v36\n"
    "v_fma_f32 v64, v64, v20, v21\n"
    "v_lshlrev_b32 v65, 16, v37\n"
    "v_fma_f32 v65, v65, v22, v23\n"
    "s_waitcnt lgkmcnt(4)\n"
    "v_lshlrev_b32 v66, 16, v38\n"
    "v_fma_f32 v66, v66, v24, v25\n"
    "v_lshlrev_b32 v67, 16, v39\n"
    "v_fma_f32 v67, v67, v26, v27\n"
    "v_cvt_pk_bf16_f32 v76, v60, v61\n"
    "v_cvt_pk_bf16_f32 v77, v62, v63\n"
    "v_cvt_pk_bf16_f32 v78, v64, v65\n"
    "v_cvt_pk_bf16_f32 v79, v66, v67\n"
    "buffer_store_dwordx4 v[76:79], v10, s[52:55], s82 offen sc1\n"
    "v_and_b32 v60, 0xffff0000, v32\n"
    "v_fma_f32 v60, v60, v12, v13\n"
    "v_and_b32 v61, 0xffff0000, v33\n"
    "v_fma_f32 v61, v61, v14, v15\n"
    "v_and_b32 v62, 0xffff0000, v34\n"
    "v_fma_f32 v62, v62, v16, v17\n"
    "v_and_b32 v63, 0xffff0000, v35\n"
    "v_fma_f32 v63, v63, v18, v19\n"
    "v_and_b32 v64, 0xffff0000, v36\n"
    "v_fma_f32 v64, v64, v20, v21\n"
    "v_and_b32 v65, 0xffff0000, v37\n"
    "v_fma_f32 v65, v65, v22, v23\n"
    "v_and_b32 v66, 0xffff0000, v38\n"
    "v_fma_f32 v66, v66, v24, v25\n"
    "v_and_b32 v67, 0xffff0000, v39\n"
    "v_fma_f32 v67, v67, v26, v27\n"
    "v_cvt_pk_bf16_f32 v80, v60, v61\n"
    "v_cvt_pk_bf16_f32 v81, v62, v63\n"
    "v_cvt_pk_bf16_f32 v82, v64, v65\n"
    "v_cvt_pk_bf16_f32 v83, v66, v67\n"
    "buffer_store_dwordx4 v[80:83], v10, s[52:55], s82 offen offset:128 sc1\n"
    "s_add_u32 s82, s82, 0x10000\n"
    "s_waitcnt lgkmcnt(3)\n"
    "v_lshlrev_b32 v60, 16, v52\n"
    "v_fma_f32 v60, v60, v12, v13\n"
    "v_lshlrev_b32 v61, 16, v53\n"
    "v_fma_f32 v61, v61, v14, v15\n"
    "s_waitcnt lgkmcnt(2)\n"
    "v_lshlrev_b32 v62, 16, v54\n"
    "v_fma_f32 v62, v62, v16, v17\n"
    "v_lshlrev_b32 v63, 16, v55\n"
    "v_fma_f32 v63, v63, v18, v19\n"
    "s_waitcnt lgkmcnt(1)\n"
    "v_lshlrev_b32 v64, 16, v56\n"
    "v_fma_f32 v64, v64, v20, v21\n"
    "v_lshlrev_b32 v65, 16, v57\n"
    "v_fma_f32 v65, v65, v22, v23\n"
    "s_waitcnt lgkmcnt(0)\n"
    "v_lshlrev_b32 v66, 16, v58\n"
    "v_fma_f32 v66, v66, v24, v25\n"
    "v_lshlrev_b32 v67, 16, v59\n"
    "v_fma_f32 v67, v67, v26, v27\n"
    "v_cvt_pk_bf16_f32 v84, v60, v61\n"
    "v_cvt_pk_bf16_f32 v85, v62, v63\n"
    "v_cvt_pk_bf16_f32 v86, v64, v65\n"
    "v_cvt_pk_bf16_f32 v87, v66, v67\n"
    "buffer_store_dwordx4 v[84:87], v10, s[52:55], s82 offen sc1\n"
    "v_and_b32 v60, 0xffff0000, v52\n"
    "v_fma_f32 v60, v60, v12, v13\n"
    "v_and_b32 v61, 0xffff0000, v53\n"
    "v_fma_f32 v61, v61, v14, v15\n"
    "v_and_b32 v62, 0xffff0000, v54\n"
    "v_fma_f32 v62, v62, v16, v17\n"
    "v_and_b32 v63, 0xffff0000, v55\n"
    "v_fma_f32 v63, v63, v18, v19\n"
    "v_and_b32 v64, 0xffff0000, v56\n"
    "v_fma_f32 v64, v64, v20, v21\n"
    "v_and_b32 v65, 0xffff0000, v57\n"
    "v_fma_f32 v65, v65, v22, v23\n"
    "v_and_b32 v66, 0xffff0000, v58\n"
    "v_fma_f32 v66, v66, v24, v25\n"
    "v_and_b32 v67, 0xffff0000, v59\n"
    "v_fma_f32 v67, v67, v26, v27\n"
    "v_cvt_pk_bf16_f32 v88, v60, v61\n"
    "v_cvt_pk_bf16_f32 v89, v62, v63\n"
    "v_cvt_pk_bf16_f32 v90, v64, v65\n"
    "v_cvt_pk_bf16_f32 v91, v66, v67\n"
    "buffer_store_dwordx4 v[88:91], v10, s[52:55], s82 offen offset:128 sc1\n"
    "s_add_u32 s82, s82, 0x10000\n"
    "s_waitcnt vmcnt(0)\n"
    "s_barrier\n"
    "s_endpgm\n"
    ".Lcsum%=:\n"
    "s_setprio 3\n"
    "v_lshlrev_b32 v4, 3, v1\n"
    "v_lshlrev_b32 v3, 2, v1\n"
    "ds_read_b64 v[52:53], v4 offset:60160\n"
    "s_lshl_b32 s74, s79, 9\n"
    "s_add_u32 s74, s74, 0x8000\n"
    "s_waitcnt lgkmcnt(0)\n"
    "buffer_store_dwordx2 v[52:53], v4, s[52:55], s74 offen sc1\n"
    "s_lshl_b32 s75, s79, 7\n"
    "s_add_u32 s75, s75, 0x1000\n"
    "s_mov_b64 exec, 1\n"
    "buffer_load_dword v63, off, s[52:55], s75 sc1\n"
    "s_mov_b64 exec, -1\n"
    "s_barrier\n"
    "s_mov_b64 exec, 1\n"
    "buffer_store_dwordx2 v[8:9], off, s[52:55], s75 sc1\n"
    "s_mov_b64 exec, -1\n"
    "s_and_b32 s74, s70, 6\n"
    "s_lshl1_add_u32 s72, s74, s71\n"
    "s_and_b32 s73, s70, 1\n"
    "s_lshl_b32 s74, s73, 4\n"
    "s_add_u32 s74, s74, s72\n"
    "s_mul_i32 s74, s74, 0xc400\n"
    "v_min_u32 v3, 55, v1\n"
    "v_mul_u32_u24 v3, 0x380, v3\n"
    "buffer_load_dword v54, v3, s[48:51], s74 offen\n"
    "buffer_load_dword v55, v3, s[48:51], s74 offen offset:128\n"
    "buffer_load_dword v56, v3, s[48:51], s74 offen offset:256\n"
    "buffer_load_dword v57, v3, s[48:51], s74 offen offset:384\n"
    "buffer_load_dword v58, v3, s[48:51], s74 offen offset:512\n"
    "buffer_load_dword v59, v3, s[48:51], s74 offen offset:640\n"
    "buffer_load_dword v60, v3, s[48:51], s74 offen offset:768\n"
    "s_cmp_lg_u32 s79, 0\n"
    "s_cbranch_scc1 .Lhdone%=\n"
    "v_lshrrev_b32 v3, 3, v1\n"
    "v_and_b32 v5, 7, v1\n"
    "v_min_u32 v5, 6, v5\n"
    "v_lshlrev_b32 v5, 7, v5\n"
    "v_lshl_add_u32 v3, v3, 13, v5\n"
    "v_and_b32 v4, 31, v1\n"
    "v_lshlrev_b32 v4, 7, v4\n"
    "s_sleep 100\n"
    "s_mov_b32 s75, 0x1000\n"
    "buffer_load_dwordx2 v[72:73], v4, s[52:55], s75 offen sc1\n"
    "s_mov_b32 s74, 0x10000\n"
    "buffer_load_dwordx2 v[64:65], v3, s[52:55], s74 offen sc1\n"
    "s_mov_b32 s74, 0x20000\n"
    "buffer_load_dwordx2 v[66:67], v3, s[52:55], s74 offen sc1\n"
    "s_mov_b32 s74, 0x30000\n"
    "buffer_load_dwordx2 v[68:69], v3, s[52:55], s74 offen sc1\n"
    "s_mov_b32 s74, 0x40000\n"
    "buffer_load_dwordx2 v[70:71], v3, s[52:55], s74 offen sc1\n"
    "v_mov_b32 v5, 0\n"
    "s_waitcnt vmcnt(3)\n"
    "v_xor_b32 v64, s76, v64\n"
    "v_xor_b32 v65, s77, v65\n"
    "v_or3_b32 v5, v5, v64, v65\n"
    "s_waitcnt vmcnt(2)\n"
    "v_xor_b32 v66, s76, v66\n"
    "v_xor_b32 v67, s77, v67\n"
    "v_or3_b32 v5, v5, v66, v67\n"
    "s_waitcnt vmcnt(1)\n"
    "v_xor_b32 v68, s76, v68\n"
    "v_xor_b32 v69, s77, v69\n"
    "v_or3_b32 v5, v5, v68, v69\n"
    "s_waitcnt vmcnt(0)\n"
    "v_xor_b32 v70, s76, v70\n"
    "v_xor_b32 v71, s77, v71\n"
    "v_or3_b32 v5, v5, v70, v71\n"
    "v_xor_b32 v72, s76, v72\n"
    "v_xor_b32 v73, s77, v73\n"
    "v_or3_b32 v5, v5, v72, v73\n"
    "v_cmp_ne_u32 vcc, 0, v5\n"
    "s_nop 0\n"
    "s_cbranch_vccnz .Lslowep%=\n"
    ".Lokep%=:\n"
    "s_mov_b64 exec, 1\n"
    "buffer_store_dwordx2 v[8:9], off, s[52:55], 0 sc1\n"
    "s_mov_b64 exec, -1\n"
    ".Lhdone%=:\n"
    "s_endpgm\n"
    ".Lend%=:\n"
    :
    : "s"(__builtin_amdgcn_kernarg_segment_ptr()), "s"(blockIdx.x), "{v0}"(threadIdx.x), "s"((__attribute__((address_space(3))) char*)lds)
    : "v1", "v2", "v3", "v4", "v5", "v6", "v7", "v8", "v9", "v10", "v11", "v12", "v13", "v14", "v15", "v16", "v17", "v18", "v19", "v20", "v21", "v22", "v23", "v24", "v25", "v26", "v27", "v28", "v29", "v30", "v31", "v32", "v33", "v34", "v35", "v36", "v37", "v38", "v39", "v40", "v41", "v42", "v43", "v44", "v45", "v46", "v47", "v48", "v49", "v50", "v51", "v52", "v53", "v54", "v55", "v56", "v57", "v58", "v59", "v60", "v61", "v62", "v63", "v64", "v65", "v66", "v67", "v68", "v69", "v70", "v71", "v72", "v73", "v74", "v75", "v76", "v77", "v78", "v79", "v80", "v81", "v82", "v83", "v84", "v85", "v86", "v87", "v88", "v89", "v90", "v91", "v92", "v93", "v94", "v95", "v96", "v97", "v98", "v99", "v100", "v101", "v102", "v103", "v104", "v105", "v106", "v107", "v108", "v109", "v110", "v111", "v112", "v113", "v114", "v115", "v116", "v117", "v118", "v119", "v120", "v121", "v122", "v123", "v124", "v125", "v126", "v127", "v128", "v129", "v130", "v131", "v132", "v133", "v134", "v135", "v136", "v137", "v138", "v139", "v140", "v141", "v142", "v143", "v144", "v145", "v146", "v147", "v148", "v149", "v150", "v151", "v152", "v153", "v154", "v155", "v156", "v157", "v158", "v159", "v160", "v161", "v162", "v163", "v164", "v165", "v166", "v167", "v168", "v169", "v170", "v171", "v172", "v173", "v174", "v175", "v176", "v177", "v178", "v179", "v180", "v181", "v182", "v183", "v184", "v185", "v186", "v187", "v188", "v189", "v190", "v191", "v192", "v193", "v194", "v195", "v196", "v197", "v198", "v199", "v200", "v201", "v202", "v203", "v204", "v205", "v206", "v207", "v208", "v209", "v210", "v211", "v212", "v213", "v214", "v215", "v216", "v217", "v218", "v219", "v220", "v221", "v222", "v223", "v224", "v225", "v226", "v227", "v228", "v229", "v230", "v231", "v232", "v233", "v234", "v235", "v236", "v237", "v238", "v239", "v240", "v241", "v242", "v243", "v244", "v245", "v246", "v247", "v248", "v249", "v250", "v251", "v252", "v253", "v254", "v255", "s8", "s9", "s10", "s11", "s12", "s13", "s14", "s15", "s16", "s17", "s18", "s19", "s20", "s21", "s22", "s23", "s24", "s25", "s26", "s27", "s28", "s29", "s30", "s31", "s32", "s33", "s34", "s35", "s36", "s37", "s38", "s39", "s40", "s41", "s42", "s43", "s44", "s45", "s46", "s47", "s48", "s49", "s50", "s51", "s52", "s53", "s54", "s55", "s56", "s57", "s58", "s59", "s60", "s61", "s62", "s63", "s64", "s65", "s66", "s67", "s68", "s69", "s70", "s71", "s72", "s73", "s74", "s75", "s76", "s77", "s78", "s79", "s80", "s81", "s82", "s83", "s84", "s85", "s86", "s87", "s88", "s89", "s90", "s91", "s92", "s93", "s94", "s95", "s96", "s97", "s98", "s99", "s100", "s101", "vcc", "scc", "memory");
}

extern "C" void kernel_launch(void* const* d_in, const int* in_sizes, int n_in, void* d_out, int out_size, void* d_ws, size_t ws_size,
                              hipStream_t stream) {
    static const int src[15] = {1, 2, 3, 4, 5, 6, 7, 8, 0, 10, 12, -1, -2, 11, 13};
    Args a;
    for (int i = 0; i < 15; ++i) {
        a.b[i].p = src[i] >= 0 ? d_in[src[i]] : (src[i] == -1 ? d_ws : d_out);
        a.b[i].bytes = src[i] >= 0 ? (unsigned)in_sizes[src[i]] * 2u : (src[i] == -1 ? WS_BYTES : (unsigned)out_size * 2u);
        a.b[i].word3 = 0x00020000u;
    }
    fused_s4_mlp<<<256, 512, 0, stream>>>(a);
}
```
